```python
import torch

def _softmax(x, axis=-1):
    return torch.softmax(x, dim=axis)
def _gelu(x):
    return torch.nn.functional.gelu(x, approximate="tanh")
def _silu(x):
    return torch.nn.functional.silu(x)
def _sigmoid(x):
    return torch.sigmoid(x)
def _layernorm(x, axis=-1, eps=1e-5):
    m = x.mean(dim=axis, keepdim=True)
    v = x.var(dim=axis, keepdim=True, unbiased=False)
    return (x - m) / torch.sqrt(v + eps)
def _scatter_add(tgt, idx, src, axis=0):
    return tgt.clone().index_add_(axis, idx, src)
def _segment_sum(src, seg, n):
    out = torch.zeros((n,) + src.shape[1:], dtype=src.dtype)
    return out.index_add_(0, seg, src)
def _pad(x, pw):
    flat = tuple(v for pair in reversed(pw) for v in pair)
    return torch.nn.functional.pad(x, flat)

kernel_name = 'sequential_preLN__sliding_local_dilated__glu_gated_swiglu__prompt'

def setup_inputs():

    _w = (lambda a: (lambda t: t.to(torch.bfloat16) if t.is_floating_point() else t)(torch.as_tensor(a)))
    def _A_setup():
        B, S, D = 1, 2048, 512
        return {
            "x": _w((lambda *s: torch.randn(*s, dtype=torch.bfloat16) * 0.05)(B, S, D)),
            "wqkv": _w((lambda *s: torch.randn(*s, dtype=torch.bfloat16) * 0.05)(D, 3 * D)),
            "wo": _w((lambda *s: torch.randn(*s, dtype=torch.bfloat16) * 0.05)(D, D)),
        }
    _A_in = _A_setup()

    def _B_setup():
        B, S, D, F = 1, 2048, 512, 1792
        return {
            "x": _w((lambda *s: torch.randn(*s, dtype=torch.bfloat16) * 0.05)(B, S, D)),
            "w1": _w((lambda *s: torch.randn(*s, dtype=torch.bfloat16) * 0.8)(D, F) * (2.0 / D) ** 0.5),
            "w3": _w((lambda *s: torch.randn(*s, dtype=torch.bfloat16) * 0.8)(D, F) * (2.0 / D) ** 0.5),
            "w2": _w((lambda *s: torch.randn(*s, dtype=torch.bfloat16) * 0.8)(F, D) * (2.0 / F) ** 0.5),
        }
    _B_in = _B_setup()

    return {
        "A_x": _A_in["x"],
        "A_wqkv": _A_in["wqkv"],
        "A_wo": _A_in["wo"],
        "B_x": _B_in["x"],
        "B_w1": _B_in["w1"],
        "B_w3": _B_in["w3"],
        "B_w2": _B_in["w2"],
    }

def reference(A_x, A_wqkv, A_wo, B_x, B_w1, B_w3, B_w2):
    def _A_ref(x, wqkv, wo):
        B, S, D = x.shape
        H = 32; Dh = D // H
        W, dil = 8, 2
        qkv = (lambda x, *d: x.permute(*d))((x @ wqkv).reshape(B, S, 3, H, Dh), 2, 0, 3, 1, 4)
        q, k, v = qkv[0], qkv[1], qkv[2]
        scale = Dh ** -0.5
        pos = torch.arange(S)
        off = torch.arange(W) * dil
        idx = pos[:, None] - off[None, :]
        mask = idx >= 0
        idx_c = torch.where(mask, idx, 0).reshape(-1)
        kg = k[:, :, idx_c, :].reshape(B, H, S, W, Dh)
        vg = v[:, :, idx_c, :].reshape(B, H, S, W, Dh)
        logits = torch.einsum("bhsd,bhswd->bhsw", q, kg) * scale
        logits = torch.where(mask[None, None, :, :], logits, -1e9)
        attn = _softmax(logits, axis=-1)
        o = torch.einsum("bhsw,bhswd->bhsd", attn, vg)
        return (lambda x, *d: x.permute(*d))(o, 0, 2, 1, 3).reshape(B, S, D) @ wo

    def _B_ref(x, w1, w3, w2):
        gate = torch.einsum("bsd,df->bsf", x, w1)
        up = torch.einsum("bsd,df->bsf", x, w3)
        h = _silu(gate) * up
        out = torch.einsum("bsf,fd->bsd", h, w2)
        return out

    x = A_x
    h = x + _A_ref(_layernorm(x, axis=-1), A_wqkv, A_wo)
    h2 = h + _B_ref(_layernorm(h, axis=-1), B_w1, B_w3, B_w2)
    return h2
```

```cpp
#include <hip/hip_runtime.h>
#include <hip/hip_bf16.h>
#include <stdint.h>
typedef unsigned short u16;
typedef short bf16x8 __attribute__((ext_vector_type(8)));
typedef float f32x4 __attribute__((ext_vector_type(4)));
__device__ __forceinline__ float bf2f(u16 b){ return __builtin_bit_cast(float,(unsigned)b<<16);}
__device__ __forceinline__ u16 f2bf(float f){ __hip_bfloat16 h=__float2bfloat16(f); return __builtin_bit_cast(u16,h);}
constexpr int S=2048,D=512,F=1792;
__global__ void k_ln(const u16* x,u16* y){
  int row=blockIdx.x, l=threadIdx.x;
  float v[8]; float s=0;
  for(int j=0;j<8;++j){v[j]=bf2f(x[row*D+l*8+j]); s+=v[j];}
  for(int o=32;o;o>>=1) s+=__shfl_xor(s,o);
  float m=s/D; float q=0;
  for(int j=0;j<8;++j){float d=v[j]-m; q+=d*d;}
  for(int o=32;o;o>>=1) q+=__shfl_xor(q,o);
  float r=rsqrtf(q/D+1e-5f);
  for(int j=0;j<8;++j) y[row*D+l*8+j]=f2bf((v[j]-m)*r);
}
__global__ void k_gemm(const u16* A,const u16* B,u16* C,const u16* R,int N,int K,int mode){
  int lane=threadIdx.x&63, w=threadIdx.x>>6;
  int m0=blockIdx.y*64+w*16, n0=blockIdx.x*16;
  f32x4 acc={0,0,0,0};
  for(int k=0;k<K;k+=32){
    bf16x8 a=*(const bf16x8*)(A+(size_t)(m0+(lane&15))*K+k+8*(lane>>4));
    bf16x8 b;
    for(int j=0;j<8;++j) b[j]=(short)B[(size_t)(k+8*(lane>>4)+j)*N+n0+(lane&15)];
    acc=__builtin_amdgcn_mfma_f32_16x16x32_bf16(a,b,acc,0,0,0);
  }
  for(int j=0;j<4;++j){ int r=m0+(lane>>4)*4+j,c=n0+(lane&15); float v=acc[j];
    if(mode==1){ v=bf2f(f2bf(v))+bf2f(R[(size_t)r*N+c]); }
    C[(size_t)r*N+c]=f2bf(v);}
}
__global__ void k_attn(const u16* qkv,u16* o){
  int s=blockIdx.x, h=threadIdx.x;
  float q[16]; for(int d=0;d<16;++d) q[d]=bf2f(qkv[(size_t)s*1536+h*16+d]);
  float lg[8]; float mx=-1e30f;
  for(int w=0;w<8;++w){ int t=s-2*w; if(t<0){lg[w]=-1e9f;} else { float a=0; for(int d=0;d<16;++d) a+=q[d]*bf2f(qkv[(size_t)t*1536+512+h*16+d]); lg[w]=a*0.25f;} mx=fmaxf(mx,lg[w]); }
  float sum=0; for(int w=0;w<8;++w){ lg[w]=__expf(lg[w]-mx); sum+=lg[w]; }
  float inv=1.f/sum;
  for(int d=0;d<16;++d){ float a=0; for(int w=0;w<8;++w){ int t=s-2*w; if(t<0)t=0; a+=lg[w]*inv*bf2f(qkv[(size_t)t*1536+1024+h*16+d]); } o[(size_t)s*D+h*16+d]=f2bf(a);}
}
__global__ void k_swiglu(const u16* g,const u16* u,u16* h,int n){ int i=blockIdx.x*256+threadIdx.x; if(i<n){ float a=bf2f(g[i]); float s=a/(1.f+__expf(-a)); h[i]=f2bf(bf2f(f2bf(s))*bf2f(u[i])); } }
extern "C" void kernel_launch(void* const* d_in,const int* in_sizes,int n_in,void* d_out,int out_size,void* d_ws,size_t ws_size,hipStream_t stream){
  const u16* x=(const u16*)d_in[0]; const u16* wqkv=(const u16*)d_in[1]; const u16* wo=(const u16*)d_in[2];
  const u16* w1=(const u16*)d_in[4]; const u16* w3=(const u16*)d_in[5]; const u16* w2=(const u16*)d_in[6];
  u16* ws=(u16*)d_ws; u16* xn=ws; u16* qkv=xn+S*D; u16* o=qkv+S*1536; u16* h=o+S*D; u16* hn=h+S*D; u16* g=hn+S*D; u16* u=g+S*F; u16* a=u+S*F;
  k_ln<<<S,64,0,stream>>>(x,xn);
  k_gemm<<<dim3(1536/16,S/64),256,0,stream>>>(xn,wqkv,qkv,nullptr,1536,D,0);
  k_attn<<<S,32,0,stream>>>(qkv,o);
  k_gemm<<<dim3(D/16,S/64),256,0,stream>>>(o,wo,h,x,D,D,1);
  k_ln<<<S,64,0,stream>>>(h,hn);
  k_gemm<<<dim3(F/16,S/64),256,0,stream>>>(hn,w1,g,nullptr,F,D,0);
  k_gemm<<<dim3(F/16,S/64),256,0,stream>>>(hn,w3,u,nullptr,F,D,0);
  k_swiglu<<<S*F/256,256,0,stream>>>(g,u,a,S*F);
  k_gemm<<<dim3(D/16,S/64),256,0,stream>>>(a,w2,(u16*)d_out,h,D,F,1);
}
```

```cpp
#include <hip/hip_runtime.h>
#include <stdint.h>

__global__ __launch_bounds__(256, 1) void fused_block_kernel(const void* x, const void* wqkv, const void* wo, const void* w1,
                                                             const void* w3, const void* w2, void* out, void* ws) {
  unsigned long long ka = (unsigned long long)__builtin_amdgcn_kernarg_segment_ptr();
  unsigned bid = blockIdx.x;
  unsigned wave = __builtin_amdgcn_readfirstlane(threadIdx.x >> 6);
  asm volatile(
"s_load_dwordx16 s[20:35], %0, 0x0\n"
"s_mov_b32 s68, %2\n"
"v_mbcnt_lo_u32_b32 v0, -1, 0\n"
"v_mbcnt_hi_u32_b32 v0, -1, v0\n"
"s_and_b32 s77, %1, 7\n"
"s_lshr_b32 s78, %1, 3\n"
"s_getpc_b64 s[98:99]\n"
".Lcode0:\n"
"s_and_b32 s79, s78, 15\n"
"s_lshl_b32 s79, s79, 2\n"
"s_add_u32 s79, s79, s68\n"
"s_lshl_b32 s79, s79, 10\n"
"v_and_b32 v18, 7, v0\n"
"v_lshl_add_u32 v18, v18, 7, s79\n"
"v_min_u32 v18, .Lcode1-.Lcode0-4, v18\n"
"global_load_dword v30, v18, s[98:99]\n"
"s_lshr_b32 s79, s77, 1\n"
"s_lshl_b32 s79, s79, 2\n"
"s_lshr_b32 s80, s78, 3\n"
"s_add_u32 s69, s79, s80\n"
"s_mov_b32 s76, s80\n"
"s_bfe_u32 s74, s78, 65538\n"
"s_and_b32 s79, s77, 1\n"
"s_lshl_b32 s79, s79, 2\n"
"s_and_b32 s80, s78, 3\n"
"s_add_u32 s71, s79, s80\n"
"s_xor_b32 s71, s71, 4\n"
"s_lshl_b32 s79, s74, 3\n"
"s_add_u32 s70, s79, s71\n"
"s_lshl_b32 s79, s69, 1\n"
"s_add_u32 s79, s79, s74\n"
"s_lshl_b32 s75, s79, 6\n"
"v_lshrrev_b32 v18, 4, v0\n"
"s_mul_i32 s79, s68, 20\n"
"v_add_u32 v18, s79, v18\n"
"s_sub_u32 s80, s75, 16\n"
"s_lshl_b32 s81, s79, 8\n"
"s_lshl_b32 s77, s76, 8\n"
"s_waitcnt lgkmcnt(0)\n"
"s_mov_b64 s[40:41], s[20:21]\n"
"s_mov_b32 s42, 0x200000\n"
"s_mov_b32 s43, 0x20000\n"
"s_cmp_eq_u32 s68, 0\n"
"s_cselect_b32 s83, 0xffff0000, -1\n"
"v_xor_b32 v19, v18, v0\n"
"v_and_b32 v19, 15, v19\n"
"v_add_u32 v11, s80, v18\n"
"v_max_i32 v11, 0, v11\n"
"v_lshlrev_b32 v11, 6, v11\n"
"v_add_lshl_u32 v11, v11, v19, 4\n"
"s_add_u32 m0, s81, 0\n"
"s_mov_b32 exec_lo, s83\n"
"buffer_load_dwordx4 v11, s[40:43], s77 offen lds\n"
"s_mov_b32 exec_lo, -1\n"
"v_add_u32 v18, 4, v18\n"
"v_xor_b32 v19, v18, v0\n"
"v_and_b32 v19, 15, v19\n"
"v_add_u32 v12, s80, v18\n"
"v_max_i32 v12, 0, v12\n"
"v_lshlrev_b32 v12, 6, v12\n"
"v_add_lshl_u32 v12, v12, v19, 4\n"
"s_add_u32 m0, s81, 1024\n"
"s_nop 0\n"
"buffer_load_dwordx4 v12, s[40:43], s77 offen lds\n"
"v_add_u32 v18, 4, v18\n"
"v_xor_b32 v19, v18, v0\n"
"v_and_b32 v19, 15, v19\n"
"v_add_u32 v13, s80, v18\n"
"v_max_i32 v13, 0, v13\n"
"v_lshlrev_b32 v13, 6, v13\n"
"v_add_lshl_u32 v13, v13, v19, 4\n"
"s_add_u32 m0, s81, 2048\n"
"s_nop 0\n"
"buffer_load_dwordx4 v13, s[40:43], s77 offen lds\n"
"v_add_u32 v18, 4, v18\n"
"v_xor_b32 v19, v18, v0\n"
"v_and_b32 v19, 15, v19\n"
"v_add_u32 v14, s80, v18\n"
"v_max_i32 v14, 0, v14\n"
"v_lshlrev_b32 v14, 6, v14\n"
"v_add_lshl_u32 v14, v14, v19, 4\n"
"s_add_u32 m0, s81, 3072\n"
"s_nop 0\n"
"buffer_load_dwordx4 v14, s[40:43], s77 offen lds\n"
"v_add_u32 v18, 4, v18\n"
"v_xor_b32 v19, v18, v0\n"
"v_and_b32 v19, 15, v19\n"
"v_add_u32 v15, s80, v18\n"
"v_max_i32 v15, 0, v15\n"
"v_lshlrev_b32 v15, 6, v15\n"
"v_add_lshl_u32 v15, v15, v19, 4\n"
"s_add_u32 m0, s81, 4096\n"
"s_nop 0\n"
"buffer_load_dwordx4 v15, s[40:43], s77 offen lds\n"
"s_mov_b64 s[44:45], s[22:23]\n"
"s_mov_b32 s46, 0x180000\n"
"s_mov_b32 s47, 0x20000\n"
"s_lshr_b32 s78, s22, 7\n"
"s_sub_u32 s78, 3, s78\n"
"s_and_b32 s78, s78, 7\n"
"s_xor_b32 s80, s78, s71\n"
"s_sub_u32 s80, s80, 1\n"
"s_lshr_b32 s79, %1, 5\n"
"s_mul_i32 s79, s79, 3\n"
"s_add_u32 s79, s79, s80\n"
"s_lshl_b32 s79, s79, 2\n"
"s_add_u32 s79, s79, s68\n"
"s_lshl_b32 s79, s79, 7\n"
"s_add_u32 s79, s79, s78\n"
"s_lshl_b32 s79, s79, 7\n"
"s_cmp_lt_u32 s80, 3\n"
"s_cselect_b32 s79, s79, 16777216\n"
"v_lshrrev_b32 v255, 4, v0\n"
"v_lshlrev_b32 v255, 24, v255\n"
"v_lshl_add_u32 v254, v0, 10, s79\n"
"v_add_u32 v254, v254, v255\n"
"buffer_load_dword v254, v254, s[44:47], 0 offen\n"
"v_lshrrev_b32 v18, 3, v0\n"
"s_lshl_b32 s79, s68, 4\n"
"v_add_u32 v18, s79, v18\n"
"v_mul_u32_u24 v16, 3072, v18\n"
"v_bfe_u32 v18, v0, 4, 1\n"
"v_lshlrev_b32 v18, 1, v18\n"
"v_xor_b32 v18, v18, v0\n"
"v_and_b32 v18, 7, v18\n"
"s_lshl_b32 s79, s71, 7\n"
"v_add_u32 v16, s79, v16\n"
"v_xor_b32 v19, 4, v18\n"
"v_lshl_add_u32 v17, v19, 4, v16\n"
"v_add_u32 v17, 24576, v17\n"
"v_lshl_add_u32 v16, v18, 4, v16\n"
"s_mul_i32 s82, s68, 6144\n"
"s_add_u32 s82, s82, 81920\n"
"s_lshl_b32 s78, s76, 1\n"
"s_xor_b32 s78, s78, 0\n"
"s_mul_i32 s78, s78, 196608\n"
"s_add_u32 m0, s82, 0\n"
"s_nop 0\n"
"buffer_load_dwordx4 v16, s[44:47], s78 offen lds\n"
"buffer_load_dwordx4 v16, s[44:47], s78 offen offset:1024 lds\n"
"buffer_load_dwordx4 v16, s[44:47], s78 offen offset:2048 lds\n"
"s_add_u32 m0, s82, 3072\n"
"s_nop 0\n"
"buffer_load_dwordx4 v17, s[44:47], s78 offen lds\n"
"buffer_load_dwordx4 v17, s[44:47], s78 offen offset:1024 lds\n"
"buffer_load_dwordx4 v17, s[44:47], s78 offen offset:2048 lds\n"
"v_mov_b32 v28, 0\n"
"global_load_dwordx2 v[28:29], v28, s[34:35] sc1\n"
"s_lshl_b32 s78, s76, 1\n"
"s_add_u32 s77, s78, 1\n"
"s_xor_b32 s77, s77, 0\n"
"s_mul_i32 s63, s77, 196608\n"
"s_add_u32 m0, s82, 24576\n"
"s_nop 0\n"
"buffer_load_dwordx4 v16, s[44:47], s63 offen lds\n"
"buffer_load_dwordx4 v16, s[44:47], s63 offen offset:1024 lds\n"
"buffer_load_dwordx4 v16, s[44:47], s63 offen offset:2048 lds\n"
"s_add_u32 m0, s82, 27648\n"
"s_nop 0\n"
"buffer_load_dwordx4 v17, s[44:47], s63 offen lds\n"
"buffer_load_dwordx4 v17, s[44:47], s63 offen offset:1024 lds\n"
"buffer_load_dwordx4 v17, s[44:47], s63 offen offset:2048 lds\n"
"s_sub_u32 s77, 0, s34\n"
"s_and_b32 s77, s77, 2097151\n"
"s_lshl_b32 s79, s69, 21\n"
"s_add_u32 s77, s77, s79\n"
"s_add_u32 s19, s77, 20971520\n"
"s_mul_i32 s77, s70, 16896\n"
"s_mul_i32 s79, s68, 128\n"
"s_add_u32 s77, s77, s79\n"
"s_lshr_b32 s93, s70, 0\n"
"s_add_u32 s93, s93, s68\n"
"s_and_b32 s79, s93, 1\n"
"s_lshl_b32 s79, s79, 13\n"
"s_add_u32 s77, s77, s79\n"
"s_xor_b32 s79, s77, 8192\n"
"s_add_u32 s80, s19, 1638400\n"
"s_add_u32 s90, s77, s80\n"
"s_add_u32 s91, s79, s80\n"
"s_mov_b32 s96, 0\n"
"s_load_dword s92, s[34:35], s90\n"
"s_load_dwordx2 s[94:95], s[34:35], s91\n"
"s_mov_b64 s[48:49], s[34:35]\n"
"s_mov_b32 s50, 0x3600000\n"
"s_mov_b32 s51, 0x20000\n"
"s_mov_b64 s[52:53], s[32:33]\n"
"s_mov_b32 s54, 0x200000\n"
"s_mov_b32 s55, 0x20000\n"
"s_lshl_b32 s78, s76, 1\n"
"s_add_u32 s77, s76, 1\n"
"s_and_b32 s77, s77, 3\n"
"s_lshl_b32 s36, s77, 8\n"
"s_getreg_b32 s97, hwreg(HW_REG_IB_STS,8,4)\n"
"s_bitset1_b32 s96, s97\n"
"s_add_u32 s77, s78, 2\n"
"s_and_b32 s77, s77, 7\n"
"s_xor_b32 s77, s77, 0\n"
"s_mul_i32 s64, s77, 196608\n"
"s_add_u32 s77, s76, 2\n"
"s_and_b32 s77, s77, 3\n"
"s_lshl_b32 s37, s77, 8\n"
"s_add_u32 s77, s78, 3\n"
"s_and_b32 s77, s77, 7\n"
"s_xor_b32 s77, s77, 0\n"
"s_getreg_b32 s97, hwreg(HW_REG_IB_STS,8,4)\n"
"s_bitset1_b32 s96, s97\n"
"s_mul_i32 s65, s77, 196608\n"
"s_add_u32 s77, s76, 3\n"
"s_and_b32 s77, s77, 3\n"
"s_lshl_b32 s38, s77, 8\n"
"s_add_u32 s77, s78, 4\n"
"s_and_b32 s77, s77, 7\n"
"s_xor_b32 s77, s77, 0\n"
"s_mul_i32 s66, s77, 196608\n"
"s_add_u32 s77, s78, 5\n"
"s_and_b32 s77, s77, 7\n"
"s_getreg_b32 s97, hwreg(HW_REG_IB_STS,8,4)\n"
"s_bitset1_b32 s96, s97\n"
"s_xor_b32 s77, s77, 0\n"
"s_mul_i32 s67, s77, 196608\n"
"s_add_u32 s77, s78, 6\n"
"s_and_b32 s77, s77, 7\n"
"s_xor_b32 s77, s77, 0\n"
"s_mul_i32 s100, s77, 196608\n"
"s_add_u32 s77, s78, 7\n"
"s_and_b32 s77, s77, 7\n"
"s_xor_b32 s77, s77, 0\n"
"s_mul_i32 s101, s77, 196608\n"
"s_getreg_b32 s97, hwreg(HW_REG_IB_STS,8,4)\n"
"s_bitset1_b32 s96, s97\n"
"v_lshlrev_b32 v1, 4, v0\n"
"v_and_b32 v201, 15, v0\n"
"v_lshrrev_b32 v202, 4, v0\n"
"s_lshl_b32 s77, 0, 3\n"
"s_add_u32 s78, s68, 1\n"
"s_lshl_b32 s78, s78, 12\n"
"v_xor_b32 v18, v201, v202\n"
"v_xor_b32 v18, s77, v18\n"
"v_lshlrev_b32 v19, 8, v201\n"
"v_xor_b32 v2, 0, v18\n"
"s_getreg_b32 s97, hwreg(HW_REG_IB_STS,8,4)\n"
"s_bitset1_b32 s96, s97\n"
"v_lshl_add_u32 v2, v2, 4, v19\n"
"v_add_u32 v6, 40960, v2\n"
"v_add_u32 v192, s78, v2\n"
"v_xor_b32 v3, 4, v18\n"
"v_lshl_add_u32 v3, v3, 4, v19\n"
"v_add_u32 v7, 40960, v3\n"
"v_add_u32 v193, s78, v3\n"
"v_xor_b32 v4, 8, v18\n"
"v_lshl_add_u32 v4, v4, 4, v19\n"
"v_add_u32 v8, 40960, v4\n"
"s_getreg_b32 s97, hwreg(HW_REG_IB_STS,8,4)\n"
"s_bitset1_b32 s96, s97\n"
"v_add_u32 v194, s78, v4\n"
"v_xor_b32 v5, 12, v18\n"
"v_lshl_add_u32 v5, v5, 4, v19\n"
"v_add_u32 v9, 40960, v5\n"
"v_add_u32 v195, s78, v5\n"
"v_mul_u32_u24 v10, 3072, v202\n"
"v_bfe_u32 v18, v0, 2, 2\n"
"v_lshl_add_u32 v10, v18, 7, v10\n"
"v_bfe_u32 v18, v0, 3, 2\n"
"v_xor_b32 v18, s68, v18\n"
"s_getreg_b32 s97, hwreg(HW_REG_IB_STS,8,4)\n"
"s_bitset1_b32 s96, s97\n"
"v_lshl_add_u32 v10, v18, 5, v10\n"
"v_and_b32 v18, 3, v0\n"
"v_lshl_add_u32 v10, v18, 3, v10\n"
"v_add_u32 v10, 81920, v10\n"
"v_mov_b32 v32, 0x3f803f80\n"
"v_mov_b32 v33, 0x3f803f80\n"
"v_mov_b32 v34, 0x3f803f80\n"
"v_mov_b32 v35, 0x3f803f80\n"
"v_mul_u32_u24 v18, 1280, v202\n"
"v_add_lshl_u32 v213, v18, v201, 4\n"
"s_getreg_b32 s97, hwreg(HW_REG_IB_STS,8,4)\n"
"s_bitset1_b32 s96, s97\n"
"v_lshlrev_b32 v212, 3, v0\n"
"s_cmp_eq_u32 s68, 3\n"
"s_cselect_b64 vcc, -1, 0\n"
"s_cselect_b32 s77, 1, 2\n"
"s_cselect_b32 s78, 1, 3\n"
"s_cselect_b64 s[84:85], s[24:25], s[26:27]\n"
"s_cselect_b64 s[86:87], s[24:25], s[28:29]\n"
"v_lshrrev_b32 v250, s77, v0\n"
"v_mul_u32_u24 v250, 3584, v250\n"
"v_and_b32 v251, s78, v0\n"
"s_getreg_b32 s97, hwreg(HW_REG_IB_STS,8,4)\n"
"s_bitset1_b32 s96, s97\n"
"v_lshl_add_u32 v250, v251, 4, v250\n"
"s_mul_i32 s77, s69, 114688\n"
"s_mul_i32 s78, s70, 224\n"
"s_add_u32 s77, s77, s78\n"
"s_lshl_b32 s78, s68, 6\n"
"s_add_u32 s77, s77, s78\n"
"v_add_u32 v18, s77, v250\n"
"v_mad_u64_u32 v[216:217], s[88:89], v18, 1, s[26:27]\n"
"v_add_u32 v18, s77, v250\n"
"v_mad_u64_u32 v[220:221], s[88:89], v18, 1, s[28:29]\n"
"s_getreg_b32 s97, hwreg(HW_REG_IB_STS,8,4)\n"
"s_bitset1_b32 s96, s97\n"
"v_lshrrev_b32 v252, 3, v0\n"
"v_lshlrev_b32 v252, 10, v252\n"
"v_and_b32 v251, 7, v0\n"
"v_lshl_add_u32 v252, v251, 4, v252\n"
"v_cndmask_b32 v251, v250, v252, vcc\n"
"s_add_u32 s77, s77, 57344\n"
"s_lshl_b32 s78, s75, 8\n"
"s_lshl_b32 s79, s71, 7\n"
"s_add_u32 s78, s78, s79\n"
"s_add_u32 s80, s78, 8192\n"
"s_getreg_b32 s97, hwreg(HW_REG_IB_STS,8,4)\n"
"s_bitset1_b32 s96, s97\n"
"s_cmp_eq_u32 s68, 3\n"
"s_cselect_b32 s78, s78, s77\n"
"s_cselect_b32 s80, s80, s77\n"
"v_add_u32 v18, s78, v251\n"
"v_mad_u64_u32 v[224:225], s[88:89], v18, 1, s[84:85]\n"
"v_add_u32 v18, s80, v251\n"
"v_mad_u64_u32 v[228:229], s[88:89], v18, 1, s[86:87]\n"
"s_mul_i32 s39, s69, 7\n"
"s_lshl_b32 s77, s74, 2\n"
"s_add_u32 s39, s39, s77\n"
"s_getreg_b32 s97, hwreg(HW_REG_IB_STS,8,4)\n"
"s_bitset1_b32 s96, s97\n"
"s_add_u32 s39, s39, s68\n"
"s_min_u32 s77, s39, 111\n"
"s_lshl_b32 s77, s77, 14\n"
"s_add_u32 s77, s77, s79\n"
"s_add_u32 s78, s77, 8192\n"
"v_add_u32 v18, s77, v252\n"
"v_mad_u64_u32 v[232:233], s[88:89], v18, 1, s[30:31]\n"
"v_add_u32 v18, s78, v252\n"
"v_mad_u64_u32 v[236:237], s[88:89], v18, 1, s[30:31]\n"
"s_mov_b32 s28, s83\n"
"s_getreg_b32 s97, hwreg(HW_REG_IB_STS,8,4)\n"
"s_bitset1_b32 s96, s97\n"
"v_bfe_u32 v250, v0, 5, 1\n"
"v_bfe_u32 v251, v0, 2, 2\n"
"v_lshl_add_u32 v250, v250, 3, v251\n"
"v_and_b32 v251, 1, v0\n"
"v_lshlrev_b32 v251, 4, v251\n"
"v_bfe_u32 v252, v0, 1, 1\n"
"v_lshl_add_u32 v251, v252, 3, v251\n"
"s_lshl_b32 s77, s68, 12\n"
"s_add_u32 s77, s77, 62464\n"
"v_add_u32 v251, s77, v251\n"
"s_getreg_b32 s97, hwreg(HW_REG_IB_STS,8,4)\n"
"s_bitset1_b32 s96, s97\n"
"v_and_b32 v252, 1, v202\n"
"v_lshl_add_u32 v248, v250, 7, v251\n"
"v_lshl_add_u32 v248, v252, 5, v248\n"
"v_lshl_add_u32 v251, v252, 10, v251\n"
"v_lshl_add_u32 v244, v250, 6, v251\n"
"v_lshl_add_u32 v240, v250, 5, v251\n"
"v_cndmask_b32 v240, v244, v240, vcc\n"
"v_cndmask_b32 v244, v244, v248, vcc\n"
"s_lshl_b32 s77, s68, 11\n"
"s_sub_u32 s77, 93184, s77\n"
"s_getreg_b32 s97, hwreg(HW_REG_IB_STS,8,4)\n"
"s_bitset1_b32 s96, s97\n"
"v_add_u32 v248, s77, v248\n"
"s_cmp_eq_u32 s68, 3\n"
"s_cselect_b32 s77, 1, 2\n"
"s_cselect_b32 s78, 512, 32\n"
"s_cselect_b32 s79, 4, 2\n"
"s_cselect_b32 s80, 64, 32\n"
"s_cselect_b32 s21, 1, 32\n"
"s_lshl_b32 s77, s77, 7\n"
"s_lshl_b32 s79, s79, 7\n"
"v_add_u32 v241, s77, v240\n"
"s_getreg_b32 s97, hwreg(HW_REG_IB_STS,8,4)\n"
"s_bitset1_b32 s96, s97\n"
"v_add_u32 v242, s78, v240\n"
"v_add_u32 v243, s78, v241\n"
"v_add_u32 v245, s79, v244\n"
"v_add_u32 v246, s80, v244\n"
"v_add_u32 v247, s80, v245\n"
"s_mul_i32 s77, s70, 7\n"
"s_lshl_b32 s78, s68, 1\n"
"s_add_u32 s77, s77, s78\n"
"s_lshl_b32 s77, s77, 4\n"
"s_add_u32 s77, s77, s69\n"
"s_getreg_b32 s97, hwreg(HW_REG_IB_STS,8,4)\n"
"s_bitset1_b32 s96, s97\n"
"s_lshl_b32 s77, s77, 11\n"
"s_add_u32 s20, s77, 3407872\n"
"s_lshl_b32 s21, s21, 10\n"
"s_add_u32 s21, s21, s20\n"
"s_add_u32 s77, s20, 1024\n"
"s_lshl_b32 s78, s71, 16\n"
"s_lshl_b32 s79, s75, 4\n"
"s_add_u32 s78, s78, s79\n"
"s_add_u32 s78, s78, 1048576\n"
"s_cmp_eq_u32 s68, 3\n"
"s_getreg_b32 s97, hwreg(HW_REG_IB_STS,8,4)\n"
"s_bitset1_b32 s96, s97\n"
"s_cselect_b32 s22, s78, s77\n"
"s_add_u32 s23, s22, 32768\n"
"s_mul_i32 s77, s71, 224\n"
"s_add_u32 s77, s77, s39\n"
"s_lshl_b32 s77, s77, 10\n"
"s_add_u32 s77, s77, 1572864\n"
"s_add_u32 s78, s68, s74\n"
"s_cmp_eq_u32 s78, 4\n"
"s_cselect_b32 s24, 1879048192, s77\n"
"s_add_u32 s25, s24, 114688\n"
"s_getreg_b32 s97, hwreg(HW_REG_IB_STS,8,4)\n"
"s_bitset1_b32 s96, s97\n"
"s_lshl_b32 s77, s70, 4\n"
"s_add_u32 s77, s77, s69\n"
"s_lshl_b32 s77, s77, 2\n"
"s_add_u32 s77, s77, s68\n"
"s_lshl_b32 s77, s77, 8\n"
"s_add_u32 s26, s77, 32768\n"
"s_waitcnt lgkmcnt(2)\n"
"s_getreg_b32 s97, hwreg(HW_REG_IB_STS,8,4)\n"
"s_bitset1_b32 s96, s97\n"
"s_bfe_u32 s77, s96, 131073\n"
"s_lshr_b32 s78, s77, 1\n"
"s_lshl_b32 s79, s77, 1\n"
"s_and_b32 s79, s79, 2\n"
"s_or_b32 s78, s78, s79\n"
"s_bitcmp1_b32 s93, 0\n"
"s_cselect_b32 s77, s77, s78\n"
"v_mov_b32 v255, s77\n"
"ds_write_b128 v213, v[32:35]\n"
"s_lshl_b32 m0, s68, 12\n"
"s_add_u32 m0, m0, 62464\n"
"s_nop 0\n"
"global_load_lds_dwordx4 v[216:217], off\n"
"s_lshl_b32 m0, s68, 12\n"
"s_add_u32 m0, m0, 63488\n"
"s_nop 0\n"
"global_load_lds_dwordx4 v[220:221], off\n"
"s_lshl_b32 m0, s68, 12\n"
"s_add_u32 m0, m0, 64512\n"
"s_nop 0\n"
"global_load_lds_dwordx4 v[224:225], off\n"
"s_lshl_b32 m0, s68, 12\n"
"s_add_u32 m0, m0, 65536\n"
"s_nop 0\n"
"global_load_lds_dwordx4 v[228:229], off\n"
"s_add_u32 m0, s81, 20480\n"
"s_mov_b32 exec_lo, s28\n"
"buffer_load_dwordx4 v11, s[40:43], s36 offen lds\n"
"s_mov_b32 exec_lo, -1\n"
"s_add_u32 m0, s81, 21504\n"
"s_nop 0\n"
"buffer_load_dwordx4 v12, s[40:43], s36 offen lds\n"
"s_add_u32 m0, s81, 22528\n"
"s_nop 0\n"
"buffer_load_dwordx4 v13, s[40:43], s36 offen lds\n"
"s_add_u32 m0, s81, 23552\n"
"s_nop 0\n"
"buffer_load_dwordx4 v14, s[40:43], s36 offen lds\n"
"s_waitcnt vmcnt(15)\n"
"s_waitcnt lgkmcnt(0)\n"
"s_barrier\n"
"ds_read_b64_tr_b16 v[144:145], v10\n"
"ds_read_b64_tr_b16 v[146:147], v10 offset:512\n"
"ds_read_b128 v[108:111], v2 offset:4096\n"
"ds_read_b64_tr_b16 v[148:149], v10 offset:1024\n"
"ds_read_b64_tr_b16 v[150:151], v10 offset:1536\n"
"ds_read_b128 v[104:107], v2\n"
"ds_read_b128 v[112:115], v2 offset:8192\n"
"ds_read_b64_tr_b16 v[152:153], v10 offset:2048\n"
"ds_read_b64_tr_b16 v[154:155], v10 offset:2560\n"
"ds_read_b128 v[116:119], v2 offset:12288\n"
"ds_read_b128 v[120:123], v2 offset:16384\n"
"ds_read_b128 v[184:187], v192\n"
"s_waitcnt lgkmcnt(10)\n"
"v_mfma_f32_16x16x32_bf16 v[52:55], v[144:147], v[32:35], 0\n"
"ds_read_b64_tr_b16 v[156:157], v10 offset:12288\n"
"s_add_u32 m0, s81, 24576\n"
"s_nop 0\n"
"buffer_load_dwordx4 v15, s[40:43], s36 offen lds\n"
"s_waitcnt lgkmcnt(10)\n"
"v_mfma_f32_16x16x32_bf16 v[36:39], v[144:147], v[108:111], 0\n"
"ds_read_b64_tr_b16 v[158:159], v10 offset:12800\n"
"s_waitcnt vmcnt(15)\n"
"s_waitcnt lgkmcnt(9)\n"
"v_mfma_f32_16x16x32_bf16 v[60:63], v[148:151], v[108:111], 0\n"
"ds_read_b128 v[128:131], v3 offset:4096\n"
"s_add_u32 m0, s82, 49152\n"
"s_nop 0\n"
"buffer_load_dwordx4 v16, s[44:47], s64 offen lds\n"
"s_waitcnt lgkmcnt(9)\n"
"v_mfma_f32_16x16x32_bf16 v[56:59], v[148:151], v[104:107], 0\n"
"ds_read_b64_tr_b16 v[160:161], v10 offset:13312\n"
"v_readfirstlane_b32 s72, v28\n"
"s_waitcnt lgkmcnt(9)\n"
"v_mfma_f32_16x16x32_bf16 v[40:43], v[144:147], v[112:115], 0\n"
"ds_read_b64_tr_b16 v[162:163], v10 offset:13824\n"
"v_readfirstlane_b32 s73, v29\n"
"v_mfma_f32_16x16x32_bf16 v[64:67], v[148:151], v[112:115], 0\n"
"ds_read_b128 v[124:127], v3\n"
"buffer_load_dwordx4 v16, s[44:47], s64 offen offset:1024 lds\n"
"s_waitcnt lgkmcnt(9)\n"
"v_mfma_f32_16x16x32_bf16 v[80:83], v[104:107], v[152:155], 0\n"
"ds_read_b128 v[132:135], v3 offset:8192\n"
"s_mul_i32 s77, s73, 1284865837\n"
"v_mfma_f32_16x16x32_bf16 v[84:87], v[108:111], v[152:155], 0\n"
"ds_read_b64_tr_b16 v[164:165], v10 offset:14336\n"
"s_mul_i32 s78, s72, 1481765933\n"
"v_mfma_f32_16x16x32_bf16 v[88:91], v[112:115], v[152:155], 0\n"
"ds_read_b64_tr_b16 v[166:167], v10 offset:14848\n"
"s_mul_hi_u32 s79, s72, 1284865837\n"
"s_waitcnt lgkmcnt(11)\n"
"v_mfma_f32_16x16x32_bf16 v[44:47], v[144:147], v[116:119], 0\n"
"ds_read_b128 v[136:139], v3 offset:12288\n"
"buffer_load_dwordx4 v16, s[44:47], s64 offen offset:2048 lds\n"
"v_mfma_f32_16x16x32_bf16 v[68:71], v[148:151], v[116:119], 0\n"
"ds_read_b128 v[140:143], v3 offset:16384\n"
"s_add_u32 s77, s77, s78\n"
"v_mfma_f32_16x16x32_bf16 v[92:95], v[116:119], v[152:155], 0\n"
"ds_read_b128 v[188:191], v193\n"
"s_add_u32 s77, s77, s79\n"
"s_waitcnt lgkmcnt(13)\n"
"v_mfma_f32_16x16x32_bf16 v[48:51], v[144:147], v[120:123], 0\n"
"s_add_u32 m0, s82, 52224\n"
"s_nop 0\n"
"buffer_load_dwordx4 v17, s[44:47], s64 offen lds\n"
"v_mfma_f32_16x16x32_bf16 v[72:75], v[148:151], v[120:123], 0\n"
"s_mul_i32 s78, s72, 1284865837\n"
"s_add_u32 s72, s78, 4150755663\n"
"s_addc_u32 s73, s77, 335903614\n"
"v_mfma_f32_16x16x32_bf16 v[96:99], v[120:123], v[152:155], 0\n"
"v_mov_b32 v28, s72\n"
"v_mov_b32 v29, s73\n"
"v_mfma_f32_16x16x32_bf16 v[172:175], v[104:107], v[104:107], 0\n"
"buffer_load_dwordx4 v17, s[44:47], s64 offen offset:1024 lds\n"
"v_xor_b32 v254, v255, v28\n"
"s_waitcnt lgkmcnt(12)\n"
"v_mfma_f32_16x16x32_bf16 v[176:179], v[32:35], v[184:187], 0\n"
"v_mov_b32 v255, v29\n"
"v_mfma_f32_16x16x32_bf16 v[180:183], v[184:187], v[184:187], 0\n"
"s_lshl_b32 s98, s70, 2\n"
"s_add_u32 s98, s98, s68\n"
"s_lshl_b32 s98, s98, 3\n"
"s_add_u32 s98, s98, s19\n"
"s_add_u32 s98, s98, 1622016\n"
"s_mov_b64 exec, 1\n"
"buffer_store_dwordx2 v[254:255], off, s[48:51], s98 sc1\n"
"s_mov_b64 exec, -1\n"
"s_waitcnt lgkmcnt(0)\n"
"s_waitcnt vmcnt(15)\n"
"s_barrier\n"
"v_mfma_f32_16x16x32_bf16 v[52:55], v[156:159], v[32:35], v[52:55]\n"
"ds_read_b64_tr_b16 v[144:145], v10 offset:24576\n"
"buffer_load_dwordx4 v17, s[44:47], s64 offen offset:2048 lds\n"
"v_mfma_f32_16x16x32_bf16 v[36:39], v[156:159], v[128:131], v[36:39]\n"
"ds_read_b64_tr_b16 v[146:147], v10 offset:25088\n"
"s_waitcnt vmcnt(14)\n"
"v_mfma_f32_16x16x32_bf16 v[60:63], v[160:163], v[128:131], v[60:63]\n"
"ds_read_b128 v[108:111], v4 offset:4096\n"
"s_add_u32 m0, s82, 0\n"
"s_nop 0\n"
"buffer_load_dwordx4 v16, s[44:47], s65 offen lds\n"
"v_mfma_f32_16x16x32_bf16 v[56:59], v[160:163], v[124:127], v[56:59]\n"
"ds_read_b64_tr_b16 v[148:149], v10 offset:25600\n"
"ds_read_b64_tr_b16 v[216:217], v240\n"
"v_mfma_f32_16x16x32_bf16 v[40:43], v[156:159], v[132:135], v[40:43]\n"
"ds_read_b64_tr_b16 v[150:151], v10 offset:26112\n"
"ds_read_b64_tr_b16 v[218:219], v241\n"
"v_mfma_f32_16x16x32_bf16 v[64:67], v[160:163], v[132:135], v[64:67]\n"
"ds_read_b128 v[104:107], v4\n"
"buffer_load_dwordx4 v16, s[44:47], s65 offen offset:1024 lds\n"
"v_mfma_f32_16x16x32_bf16 v[80:83], v[124:127], v[164:167], v[80:83]\n"
"ds_read_b128 v[112:115], v4 offset:8192\n"
"ds_read_b64_tr_b16 v[220:221], v242\n"
"v_mfma_f32_16x16x32_bf16 v[84:87], v[128:131], v[164:167], v[84:87]\n"
"ds_read_b64_tr_b16 v[152:153], v10 offset:26624\n"
"ds_read_b64_tr_b16 v[222:223], v243\n"
"v_mfma_f32_16x16x32_bf16 v[88:91], v[132:135], v[164:167], v[88:91]\n"
"ds_read_b64_tr_b16 v[154:155], v10 offset:27136\n"
"s_waitcnt vmcnt(14)\n"
"v_mfma_f32_16x16x32_bf16 v[44:47], v[156:159], v[136:139], v[44:47]\n"
"ds_read_b128 v[116:119], v4 offset:12288\n"
"buffer_load_dwordx4 v16, s[44:47], s65 offen offset:2048 lds\n"
"v_mfma_f32_16x16x32_bf16 v[68:71], v[160:163], v[136:139], v[68:71]\n"
"ds_read_b128 v[120:123], v4 offset:16384\n"
"ds_read_b64_tr_b16 v[224:225], v244 offset:2048\n"
"v_mfma_f32_16x16x32_bf16 v[92:95], v[136:139], v[164:167], v[92:95]\n"
"ds_read_b128 v[184:187], v194\n"
"ds_read_b64_tr_b16 v[226:227], v245 offset:2048\n"
"v_mfma_f32_16x16x32_bf16 v[48:51], v[156:159], v[140:143], v[48:51]\n"
"s_add_u32 m0, s82, 3072\n"
"s_nop 0\n"
"buffer_load_dwordx4 v17, s[44:47], s65 offen lds\n"
"v_mfma_f32_16x16x32_bf16 v[72:75], v[160:163], v[140:143], v[72:75]\n"
"ds_read_b64_tr_b16 v[228:229], v246 offset:2048\n"
"ds_read_b64_tr_b16 v[230:231], v247 offset:2048\n"
"v_mfma_f32_16x16x32_bf16 v[96:99], v[140:143], v[164:167], v[96:99]\n"
"v_and_b32 v209, 1, v202\n"
"v_lshrrev_b32 v18, 1, v202\n"
"v_mfma_f32_16x16x32_bf16 v[172:175], v[124:127], v[124:127], v[172:175]\n"
"s_waitcnt lgkmcnt(13)\n"
"buffer_store_dwordx4 v[216:219], v1, s[48:51], s20 offen sc1\n"
"v_lshl_add_u32 v209, v209, 1, v18\n"
"v_mfma_f32_16x16x32_bf16 v[176:179], v[32:35], v[188:191], v[176:179]\n"
"v_lshl_add_u32 v209, v201, 2, v209\n"
"v_lshlrev_b32 v209, 2, v209\n"
"v_mfma_f32_16x16x32_bf16 v[180:183], v[188:191], v[188:191], v[180:183]\n"
"s_lshr_b32 s31, s68, 1\n"
"v_mfma_f32_32x32x16_bf16 a[64:79], v[32:35], v[216:219], 0\n"
"s_waitcnt lgkmcnt(3)\n"
"v_mfma_f32_16x16x32_bf16 v[52:55], v[144:147], v[32:35], v[52:55]\n"
"ds_read_b64_tr_b16 v[156:157], v10 offset:36864\n"
"v_mfma_f32_16x16x32_bf16 v[36:39], v[144:147], v[108:111], v[36:39]\n"
"ds_read_b64_tr_b16 v[158:159], v10 offset:37376\n"
"buffer_load_dwordx4 v17, s[44:47], s65 offen offset:1024 lds\n"
"v_mfma_f32_16x16x32_bf16 v[60:63], v[148:151], v[108:111], v[60:63]\n"
"ds_read_b128 v[128:131], v5 offset:4096\n"
"v_mfma_f32_32x32x16_bf16 a[80:95], v[32:35], v[220:223], 0\n"
"v_mfma_f32_16x16x32_bf16 v[56:59], v[148:151], v[104:107], v[56:59]\n"
"ds_read_b64_tr_b16 v[160:161], v10 offset:37888\n"
"s_waitcnt lgkmcnt(6)\n"
"v_mfma_f32_32x32x16_bf16 a[96:111], v[32:35], v[224:227], 0\n"
"v_mfma_f32_16x16x32_bf16 v[40:43], v[144:147], v[112:115], v[40:43]\n"
"ds_read_b64_tr_b16 v[162:163], v10 offset:38400\n"
"s_waitcnt lgkmcnt(5)\n"
"v_mfma_f32_32x32x16_bf16 a[112:127], v[32:35], v[228:231], 0\n"
"v_mfma_f32_16x16x32_bf16 v[64:67], v[148:151], v[112:115], v[64:67]\n"
"ds_read_b128 v[124:127], v5\n"
"buffer_load_dwordx4 v17, s[44:47], s65 offen offset:2048 lds\n"
"v_mfma_f32_16x16x32_bf16 v[80:83], v[104:107], v[152:155], v[80:83]\n"
"ds_read_b128 v[132:135], v5 offset:8192\n"
"s_lshr_b32 s77, s71, 1\n"
"v_mfma_f32_16x16x32_bf16 v[84:87], v[108:111], v[152:155], v[84:87]\n"
"ds_read_b64_tr_b16 v[164:165], v10 offset:38912\n"
"s_lshl_b32 s78, s74, 3\n"
"v_mfma_f32_16x16x32_bf16 v[88:91], v[112:115], v[152:155], v[88:91]\n"
"ds_read_b64_tr_b16 v[166:167], v10 offset:39424\n"
"s_add_u32 s77, s77, s78\n"
"v_mfma_f32_16x16x32_bf16 v[44:47], v[144:147], v[116:119], v[44:47]\n"
"ds_read_b128 v[136:139], v5 offset:12288\n"
"s_lshl_b32 s77, s77, 14\n"
"v_mfma_f32_16x16x32_bf16 v[68:71], v[148:151], v[116:119], v[68:71]\n"
"ds_read_b128 v[140:143], v5 offset:16384\n"
"s_and_b32 s78, s71, 1\n"
"v_mfma_f32_16x16x32_bf16 v[92:95], v[116:119], v[152:155], v[92:95]\n"
"ds_read_b128 v[188:191], v195\n"
"s_lshl_b32 s78, s78, 12\n"
"v_mfma_f32_16x16x32_bf16 v[48:51], v[144:147], v[120:123], v[48:51]\n"
"v_accvgpr_read_b32 v240, a64\n"
"v_accvgpr_read_b32 v241, a80\n"
"v_mfma_f32_16x16x32_bf16 v[72:75], v[148:151], v[120:123], v[72:75]\n"
"v_accvgpr_read_b32 v242, a96\n"
"v_accvgpr_read_b32 v243, a112\n"
"v_mfma_f32_16x16x32_bf16 v[96:99], v[120:123], v[152:155], v[96:99]\n"
"v_cndmask_b32 v242, v242, v241, vcc\n"
"v_add_f32 v241, v241, v243\n"
"v_mfma_f32_16x16x32_bf16 v[172:175], v[104:107], v[104:107], v[172:175]\n"
"v_add_f32 v240, v240, v242\n"
"s_nop 1\n"
"v_permlane32_swap_b32 v240, v241\n"
"v_mfma_f32_16x16x32_bf16 v[176:179], v[32:35], v[184:187], v[176:179]\n"
"buffer_store_dwordx4 v[220:223], v1, s[48:51], s21 offen sc1\n"
"buffer_store_dword v240, v209, s[48:51], s26 offen sc1\n"
"v_mfma_f32_16x16x32_bf16 v[180:183], v[184:187], v[184:187], v[180:183]\n"
"s_add_u32 s77, s77, s78\n"
"s_add_u32 s77, s77, s19\n"
"s_waitcnt lgkmcnt(0)\n"
"s_waitcnt vmcnt(9)\n"
"s_barrier\n"
"v_mfma_f32_16x16x32_bf16 v[52:55], v[156:159], v[32:35], v[52:55]\n"
"ds_read_b64_tr_b16 v[144:145], v10 offset:49152\n"
"s_add_u32 m0, s82, 24576\n"
"s_nop 0\n"
"buffer_load_dwordx4 v16, s[44:47], s66 offen lds\n"
"v_mfma_f32_16x16x32_bf16 v[36:39], v[156:159], v[128:131], v[36:39]\n"
"ds_read_b64_tr_b16 v[146:147], v10 offset:49664\n"
"s_lshl_b32 s78, s68, 10\n"
"v_mfma_f32_16x16x32_bf16 v[60:63], v[160:163], v[128:131], v[60:63]\n"
"ds_read_b128 v[108:111], v2 offset:24576\n"
"buffer_load_dwordx4 v16, s[44:47], s66 offen offset:1024 lds\n"
"v_mfma_f32_16x16x32_bf16 v[56:59], v[160:163], v[124:127], v[56:59]\n"
"ds_read_b64_tr_b16 v[148:149], v10 offset:50176\n"
"s_add_u32 s57, s77, s78\n"
"v_mfma_f32_16x16x32_bf16 v[40:43], v[156:159], v[132:135], v[40:43]\n"
"ds_read_b64_tr_b16 v[150:151], v10 offset:50688\n"
"s_add_u32 s58, s57, 65536\n"
"v_mfma_f32_16x16x32_bf16 v[64:67], v[160:163], v[132:135], v[64:67]\n"
"ds_read_b128 v[104:107], v2 offset:20480\n"
"s_lshl_b32 s78, s31, 16\n"
"v_mfma_f32_16x16x32_bf16 v[80:83], v[124:127], v[164:167], v[80:83]\n"
"ds_read_b128 v[112:115], v2 offset:28672\n"
"buffer_load_dwordx4 v16, s[44:47], s66 offen offset:2048 lds\n"
"v_mfma_f32_16x16x32_bf16 v[84:87], v[128:131], v[164:167], v[84:87]\n"
"ds_read_b64_tr_b16 v[152:153], v10 offset:51200\n"
"s_add_u32 s77, s77, s78\n"
"v_mfma_f32_16x16x32_bf16 v[88:91], v[132:135], v[164:167], v[88:91]\n"
"ds_read_b64_tr_b16 v[154:155], v10 offset:51712\n"
"s_and_b32 s78, s68, 1\n"
"v_mfma_f32_16x16x32_bf16 v[44:47], v[156:159], v[136:139], v[44:47]\n"
"ds_read_b128 v[116:119], v2 offset:32768\n"
"s_add_u32 m0, s82, 27648\n"
"s_nop 0\n"
"buffer_load_dwordx4 v17, s[44:47], s66 offen lds\n"
"v_mfma_f32_16x16x32_bf16 v[68:71], v[160:163], v[136:139], v[68:71]\n"
"ds_read_b128 v[120:123], v2 offset:36864\n"
"s_lshl_b32 s79, s78, 11\n"
"v_mfma_f32_16x16x32_bf16 v[92:95], v[136:139], v[164:167], v[92:95]\n"
"ds_read_b128 v[184:187], v192 offset:20480\n"
"s_add_u32 s77, s77, s79\n"
"v_mfma_f32_16x16x32_bf16 v[48:51], v[156:159], v[140:143], v[48:51]\n"
"s_add_u32 s29, s77, 262144\n"
"s_lshl_b32 s77, s74, 2\n"
"v_mfma_f32_16x16x32_bf16 v[72:75], v[160:163], v[140:143], v[72:75]\n"
"buffer_load_dwordx4 v17, s[44:47], s66 offen offset:1024 lds\n"
"s_add_u32 s77, s77, s31\n"
"v_mfma_f32_16x16x32_bf16 v[96:99], v[140:143], v[164:167], v[96:99]\n"
"s_lshl_b32 s77, s77, 3\n"
"s_add_u32 s77, s77, s71\n"
"v_mfma_f32_16x16x32_bf16 v[172:175], v[124:127], v[124:127], v[172:175]\n"
"s_lshl_b32 s77, s77, 1\n"
"s_add_u32 s77, s77, s78\n"
"v_mfma_f32_16x16x32_bf16 v[176:179], v[32:35], v[188:191], v[176:179]\n"
"buffer_store_dwordx4 v[224:227], v1, s[48:51], s22 offen sc1\n"
"s_lshl_b32 s77, s77, 8\n"
"v_mfma_f32_16x16x32_bf16 v[180:183], v[188:191], v[188:191], v[180:183]\n"
"s_add_u32 s77, s77, s19\n"
"s_add_u32 s30, s77, 1572864\n"
"s_waitcnt lgkmcnt(0)\n"
"v_mfma_f32_16x16x32_bf16 v[52:55], v[144:147], v[32:35], v[52:55]\n"
"ds_read_b64_tr_b16 v[156:157], v10 offset:61440\n"
"v_mfma_f32_16x16x32_bf16 v[36:39], v[144:147], v[108:111], v[36:39]\n"
"ds_read_b64_tr_b16 v[158:159], v10 offset:61952\n"
"v_accvgpr_write_b32 a224, -1\n"
"v_mfma_f32_16x16x32_bf16 v[60:63], v[148:151], v[108:111], v[60:63]\n"
"ds_read_b128 v[128:131], v3 offset:24576\n"
"buffer_load_dwordx4 v17, s[44:47], s66 offen offset:2048 lds\n"
"v_mfma_f32_16x16x32_bf16 v[56:59], v[148:151], v[104:107], v[56:59]\n"
"ds_read_b64_tr_b16 v[160:161], v10 offset:62464\n"
"v_accvgpr_write_b32 a225, -1\n"
"v_mfma_f32_16x16x32_bf16 v[40:43], v[144:147], v[112:115], v[40:43]\n"
"ds_read_b64_tr_b16 v[162:163], v10 offset:62976\n"
"v_accvgpr_write_b32 a226, -1\n"
"v_mfma_f32_16x16x32_bf16 v[64:67], v[148:151], v[112:115], v[64:67]\n"
"ds_read_b128 v[124:127], v3 offset:20480\n"
"s_add_u32 m0, s81, 40960\n"
"s_mov_b32 exec_lo, s28\n"
"buffer_load_dwordx4 v11, s[40:43], s37 offen lds\n"
"s_mov_b32 exec_lo, -1\n"
"v_mfma_f32_16x16x32_bf16 v[80:83], v[104:107], v[152:155], v[80:83]\n"
"ds_read_b128 v[132:135], v3 offset:28672\n"
"v_accvgpr_write_b32 a227, -1\n"
"v_mfma_f32_16x16x32_bf16 v[84:87], v[108:111], v[152:155], v[84:87]\n"
"ds_read_b64_tr_b16 v[164:165], v10 offset:63488\n"
"s_lshl_b32 s77, s71, 8\n"
"v_mfma_f32_16x16x32_bf16 v[88:91], v[112:115], v[152:155], v[88:91]\n"
"ds_read_b64_tr_b16 v[166:167], v10 offset:64000\n"
"s_add_u32 m0, s81, 41984\n"
"s_nop 0\n"
"buffer_load_dwordx4 v12, s[40:43], s37 offen lds\n"
"v_mfma_f32_16x16x32_bf16 v[44:47], v[144:147], v[116:119], v[44:47]\n"
"ds_read_b128 v[136:139], v3 offset:32768\n"
"s_lshr_b32 s78, s75, 3\n"
"v_mfma_f32_16x16x32_bf16 v[68:71], v[148:151], v[116:119], v[68:71]\n"
"ds_read_b128 v[140:143], v3 offset:36864\n"
"s_add_u32 s77, s77, 19398656\n"
"v_mfma_f32_16x16x32_bf16 v[92:95], v[116:119], v[152:155], v[92:95]\n"
"ds_read_b128 v[188:191], v193 offset:20480\n"
"s_add_u32 m0, s81, 43008\n"
"s_nop 0\n"
"buffer_load_dwordx4 v13, s[40:43], s37 offen lds\n"
"v_mfma_f32_16x16x32_bf16 v[48:51], v[144:147], v[120:123], v[48:51]\n"
"s_add_u32 s27, s78, s77\n"
"v_and_b32 v253, 31, v0\n"
"v_mfma_f32_16x16x32_bf16 v[72:75], v[148:151], v[120:123], v[72:75]\n"
"v_lshl_add_u32 v253, v253, 3, s77\n"
"s_lshl_b32 s78, s70, 3\n"
"v_mfma_f32_16x16x32_bf16 v[96:99], v[120:123], v[152:155], v[96:99]\n"
"s_add_u32 m0, s81, 44032\n"
"s_nop 0\n"
"buffer_load_dwordx4 v14, s[40:43], s37 offen lds\n"
"v_mfma_f32_16x16x32_bf16 v[172:175], v[104:107], v[104:107], v[172:175]\n"
"s_add_u32 s78, s78, s19\n"
"s_add_u32 s18, s78, 1605632\n"
"v_mfma_f32_16x16x32_bf16 v[176:179], v[32:35], v[184:187], v[176:179]\n"
"buffer_store_dwordx4 v[228:231], v1, s[48:51], s23 offen sc1\n"
"s_add_u32 s77, s19, 1603840\n"
"v_mfma_f32_16x16x32_bf16 v[180:183], v[184:187], v[184:187], v[180:183]\n"
"v_lshl_add_u32 v18, v201, 3, s77\n"
"v_cmp_gt_u32 s[84:85], 32, v0\n"
"s_waitcnt lgkmcnt(0)\n"
"s_waitcnt vmcnt(14)\n"
"s_barrier\n"
"v_mfma_f32_16x16x32_bf16 v[52:55], v[156:159], v[32:35], v[52:55]\n"
"ds_read_b64_tr_b16 v[144:145], v10\n"
"v_mfma_f32_16x16x32_bf16 v[36:39], v[156:159], v[128:131], v[36:39]\n"
"ds_read_b64_tr_b16 v[146:147], v10 offset:512\n"
"v_cndmask_b32 v208, v18, v253, s[84:85]\n"
"v_mfma_f32_16x16x32_bf16 v[60:63], v[160:163], v[128:131], v[60:63]\n"
"ds_read_b128 v[108:111], v4 offset:24576\n"
"s_lshl_b32 s79, s68, 4\n"
"v_mfma_f32_16x16x32_bf16 v[56:59], v[160:163], v[124:127], v[56:59]\n"
"ds_read_b64_tr_b16 v[148:149], v10 offset:1024\n"
"s_add_u32 m0, s81, 45056\n"
"s_nop 0\n"
"buffer_load_dwordx4 v15, s[40:43], s37 offen lds\n"
"v_mfma_f32_16x16x32_bf16 v[40:43], v[156:159], v[132:135], v[40:43]\n"
"ds_read_b64_tr_b16 v[150:151], v10 offset:1536\n"
"s_add_u32 s79, s79, s70\n"
"v_mfma_f32_16x16x32_bf16 v[64:67], v[160:163], v[132:135], v[64:67]\n"
"ds_read_b128 v[104:107], v4 offset:20480\n"
"s_lshl_b32 s79, s79, 14\n"
"v_mfma_f32_16x16x32_bf16 v[80:83], v[124:127], v[164:167], v[80:83]\n"
"ds_read_b128 v[112:115], v4 offset:28672\n"
"s_add_u32 m0, s82, 49152\n"
"s_nop 0\n"
"buffer_load_dwordx4 v16, s[44:47], s67 offen lds\n"
"v_mfma_f32_16x16x32_bf16 v[84:87], v[128:131], v[164:167], v[84:87]\n"
"ds_read_b64_tr_b16 v[152:153], v10 offset:2048\n"
"s_add_u32 s79, s79, s19\n"
"v_mfma_f32_16x16x32_bf16 v[88:91], v[132:135], v[164:167], v[88:91]\n"
"ds_read_b64_tr_b16 v[154:155], v10 offset:2560\n"
"s_add_u32 s39, s79, 524288\n"
"v_mfma_f32_16x16x32_bf16 v[44:47], v[156:159], v[136:139], v[44:47]\n"
"ds_read_b128 v[116:119], v4 offset:32768\n"
"buffer_load_dwordx4 v16, s[44:47], s67 offen offset:1024 lds\n"
"v_mfma_f32_16x16x32_bf16 v[68:71], v[160:163], v[136:139], v[68:71]\n"
"ds_read_b128 v[120:123], v4 offset:36864\n"
"v_add_u32 v30, 4096, v1\n"
"v_mfma_f32_16x16x32_bf16 v[92:95], v[136:139], v[164:167], v[92:95]\n"
"ds_read_b128 v[184:187], v194 offset:20480\n"
"s_and_b32 s77, s68, 1\n"
"v_mfma_f32_16x16x32_bf16 v[48:51], v[156:159], v[140:143], v[48:51]\n"
"s_lshl_b32 s78, s71, 1\n"
"s_add_u32 s77, s77, s78\n"
"v_mfma_f32_16x16x32_bf16 v[72:75], v[160:163], v[140:143], v[72:75]\n"
"buffer_load_dwordx4 v16, s[44:47], s67 offen offset:2048 lds\n"
"s_lshl_b32 s77, s77, 15\n"
"v_mfma_f32_16x16x32_bf16 v[96:99], v[140:143], v[164:167], v[96:99]\n"
"s_lshl_b32 s78, s68, 13\n"
"s_add_u32 s77, s77, s78\n"
"v_mfma_f32_16x16x32_bf16 v[172:175], v[124:127], v[124:127], v[172:175]\n"
"s_and_b32 s78, s69, 0\n"
"s_lshl_b32 s78, s78, 12\n"
"v_mfma_f32_16x16x32_bf16 v[176:179], v[32:35], v[188:191], v[176:179]\n"
"s_add_u32 m0, s82, 52224\n"
"s_nop 0\n"
"buffer_load_dwordx4 v17, s[44:47], s67 offen lds\n"
"v_mfma_f32_16x16x32_bf16 v[180:183], v[188:191], v[188:191], v[180:183]\n"
"s_add_u32 s77, s77, s78\n"
"s_add_u32 s32, s77, 1048576\n"
"s_waitcnt lgkmcnt(0)\n"
"v_mfma_f32_16x16x32_bf16 v[52:55], v[144:147], v[32:35], v[52:55]\n"
"ds_read_b64_tr_b16 v[156:157], v10 offset:12288\n"
"v_mfma_f32_16x16x32_bf16 v[36:39], v[144:147], v[108:111], v[36:39]\n"
"ds_read_b64_tr_b16 v[158:159], v10 offset:12800\n"
"s_xor_b32 s33, s32, 4096\n"
"v_mfma_f32_16x16x32_bf16 v[60:63], v[148:151], v[108:111], v[60:63]\n"
"ds_read_b128 v[128:131], v5 offset:24576\n"
"buffer_load_dwordx4 v17, s[44:47], s67 offen offset:1024 lds\n"
"v_mfma_f32_16x16x32_bf16 v[56:59], v[148:151], v[104:107], v[56:59]\n"
"ds_read_b64_tr_b16 v[160:161], v10 offset:13312\n"
"s_xor_b32 s34, s32, 32768\n"
"v_mfma_f32_16x16x32_bf16 v[40:43], v[144:147], v[112:115], v[40:43]\n"
"ds_read_b64_tr_b16 v[162:163], v10 offset:13824\n"
"s_xor_b32 s35, s34, 4096\n"
"v_mfma_f32_16x16x32_bf16 v[64:67], v[148:151], v[112:115], v[64:67]\n"
"ds_read_b128 v[124:127], v5 offset:20480\n"
"buffer_load_dwordx4 v17, s[44:47], s67 offen offset:2048 lds\n"
"v_mfma_f32_16x16x32_bf16 v[80:83], v[104:107], v[152:155], v[80:83]\n"
"ds_read_b128 v[132:135], v5 offset:28672\n"
"s_lshl_b32 s77, s70, 14\n"
"v_mfma_f32_16x16x32_bf16 v[84:87], v[108:111], v[152:155], v[84:87]\n"
"ds_read_b64_tr_b16 v[164:165], v10 offset:14336\n"
"s_lshl_b32 s78, s68, 8\n"
"v_mfma_f32_16x16x32_bf16 v[88:91], v[112:115], v[152:155], v[88:91]\n"
"ds_read_b64_tr_b16 v[166:167], v10 offset:14848\n"
"s_add_u32 s77, s77, s78\n"
"v_mfma_f32_16x16x32_bf16 v[44:47], v[144:147], v[116:119], v[44:47]\n"
"ds_read_b128 v[136:139], v5 offset:32768\n"
"s_lshl_b32 m0, s68, 11\n"
"s_add_u32 m0, m0, 155648\n"
"s_nop 0\n"
"global_load_lds_dwordx4 v[232:233], off\n"
"v_mfma_f32_16x16x32_bf16 v[68:71], v[148:151], v[116:119], v[68:71]\n"
"ds_read_b128 v[140:143], v5 offset:36864\n"
"s_add_u32 s56, s77, 32768\n"
"v_mfma_f32_16x16x32_bf16 v[92:95], v[116:119], v[152:155], v[92:95]\n"
"ds_read_b128 v[188:191], v195 offset:20480\n"
"s_mov_b32 s62, 0\n"
"v_mfma_f32_16x16x32_bf16 v[48:51], v[144:147], v[120:123], v[48:51]\n"
"s_add_u32 s98, s19, 1622016\n"
"v_add_u32 v212, s98, v212\n"
"s_cmp_eq_u32 s68, 2\n"
"s_cselect_b64 exec, -1, 0\n"
"buffer_load_dwordx2 v[206:207], v212, s[48:51], 0 offen sc1\n"
"s_mov_b64 exec, -1\n"
"s_lshl_b32 m0, s68, 11\n"
"s_add_u32 m0, m0, 156672\n"
"s_nop 0\n"
"global_load_lds_dwordx4 v[236:237], off\n"
"v_mfma_f32_16x16x32_bf16 v[72:75], v[148:151], v[120:123], v[72:75]\n"
"s_add_u32 s77, s62, s68\n"
"s_and_b32 s77, s77, 7\n"
"v_mfma_f32_16x16x32_bf16 v[96:99], v[120:123], v[152:155], v[96:99]\n"
"s_lshl_b32 s77, s77, 12\n"
"s_lshl_b32 s62, s62, 12\n"
"v_mfma_f32_16x16x32_bf16 v[172:175], v[104:107], v[104:107], v[172:175]\n"
"s_mul_i32 s78, s70, 229376\n"
"s_add_u32 s78, s78, 3407872\n"
"v_mfma_f32_16x16x32_bf16 v[176:179], v[32:35], v[184:187], v[176:179]\n"
"s_add_u32 s59, s78, s77\n"
"s_xor_b32 s60, s59, 16384\n"
"v_mfma_f32_16x16x32_bf16 v[180:183], v[184:187], v[184:187], v[180:183]\n"
"s_lshl_b32 s61, s68, 12\n"
"v_lshlrev_b32 v203, 2, v201\n"
"s_waitcnt lgkmcnt(0)\n"
"s_waitcnt vmcnt(15)\n"
"s_waitcnt vmcnt(9)\n"
"s_barrier\n"
"v_mfma_f32_16x16x32_bf16 v[52:55], v[156:159], v[32:35], v[52:55]\n"
"ds_read_b64_tr_b16 v[144:145], v10 offset:24576\n"
"s_cmp_lg_u32 s68, 1\n"
"s_cbranch_scc1 .Lpub1\n"
"s_mov_b64 exec, 1\n"
"buffer_store_dwordx2 v[28:29], off, s[48:51], s27 sc1\n"
"s_mov_b64 exec, -1\n"
".Lpub1:\n"
"s_add_u32 m0, s82, 0\n"
"s_nop 0\n"
"buffer_load_dwordx4 v16, s[44:47], s100 offen lds\n"
"v_mfma_f32_16x16x32_bf16 v[36:39], v[156:159], v[128:131], v[36:39]\n"
"ds_read_b64_tr_b16 v[146:147], v10 offset:25088\n"
"buffer_load_dwordx4 v16, s[44:47], s100 offen offset:1024 lds\n"
"v_mfma_f32_16x16x32_bf16 v[60:63], v[160:163], v[128:131], v[60:63]\n"
"ds_read_b128 v[108:111], v6 offset:4096\n"
"v_add_u32 v203, 155648, v203\n"
"v_mfma_f32_16x16x32_bf16 v[56:59], v[160:163], v[124:127], v[56:59]\n"
"ds_read_b64_tr_b16 v[148:149], v10 offset:25600\n"
"buffer_load_dwordx4 v16, s[44:47], s100 offen offset:2048 lds\n"
"v_mfma_f32_16x16x32_bf16 v[40:43], v[156:159], v[132:135], v[40:43]\n"
"ds_read_b64_tr_b16 v[150:151], v10 offset:26112\n"
"s_add_u32 s77, s68, 1\n"
"v_mfma_f32_16x16x32_bf16 v[64:67], v[160:163], v[132:135], v[64:67]\n"
"ds_read_b128 v[104:107], v6\n"
"s_lshl_b32 s77, s77, 6\n"
"v_mfma_f32_16x16x32_bf16 v[80:83], v[124:127], v[164:167], v[80:83]\n"
"ds_read_b128 v[112:115], v6 offset:8192\n"
"v_add_u32 v204, s77, v203\n"
"v_mfma_f32_16x16x32_bf16 v[84:87], v[128:131], v[164:167], v[84:87]\n"
"ds_read_b64_tr_b16 v[152:153], v10 offset:26624\n"
"s_add_u32 m0, s82, 3072\n"
"s_nop 0\n"
"buffer_load_dwordx4 v17, s[44:47], s100 offen lds\n"
"v_mfma_f32_16x16x32_bf16 v[88:91], v[132:135], v[164:167], v[88:91]\n"
"ds_read_b64_tr_b16 v[154:155], v10 offset:27136\n"
"v_lshlrev_b32 v205, 4, v202\n"
"v_mfma_f32_16x16x32_bf16 v[44:47], v[156:159], v[136:139], v[44:47]\n"
"ds_read_b128 v[116:119], v6 offset:12288\n"
"v_add_u32 v205, 155648, v205\n"
"v_mfma_f32_16x16x32_bf16 v[68:71], v[160:163], v[136:139], v[68:71]\n"
"ds_read_b128 v[120:123], v6 offset:16384\n"
"buffer_load_dwordx4 v17, s[44:47], s100 offen offset:1024 lds\n"
"v_mfma_f32_16x16x32_bf16 v[92:95], v[136:139], v[164:167], v[92:95]\n"
"ds_read_b128 v[184:187], v192 offset:40960\n"
"s_lshr_b32 s77, s71, 1\n"
"v_mfma_f32_16x16x32_bf16 v[48:51], v[156:159], v[140:143], v[48:51]\n"
"s_sub_u32 s77, s77, s76\n"
"s_and_b32 s77, s77, 3\n"
"v_mfma_f32_16x16x32_bf16 v[72:75], v[160:163], v[140:143], v[72:75]\n"
"s_mul_i32 s77, s77, 20480\n"
"s_lshl_b32 s78, s31, 13\n"
"v_mfma_f32_16x16x32_bf16 v[96:99], v[140:143], v[164:167], v[96:99]\n"
"buffer_load_dwordx4 v17, s[44:47], s100 offen offset:2048 lds\n"
"s_add_u32 s77, s77, s78\n"
"v_mfma_f32_16x16x32_bf16 v[172:175], v[124:127], v[124:127], v[172:175]\n"
"s_add_u32 s77, s77, 4096\n"
"s_and_b32 s78, s71, 1\n"
"v_mfma_f32_16x16x32_bf16 v[176:179], v[32:35], v[188:191], v[176:179]\n"
"s_lshl_b32 s78, s78, 1\n"
"s_and_b32 s79, s68, 1\n"
"v_mfma_f32_16x16x32_bf16 v[180:183], v[188:191], v[188:191], v[180:183]\n"
"s_add_u32 m0, s81, 61440\n"
"s_mov_b32 exec_lo, s28\n"
"buffer_load_dwordx4 v11, s[40:43], s38 offen lds\n"
"s_mov_b32 exec_lo, -1\n"
"s_waitcnt lgkmcnt(0)\n"
"v_mfma_f32_16x16x32_bf16 v[52:55], v[144:147], v[32:35], v[52:55]\n"
"ds_read_b64_tr_b16 v[156:157], v10 offset:36864\n"
"v_mfma_f32_16x16x32_bf16 v[36:39], v[144:147], v[108:111], v[36:39]\n"
"ds_read_b64_tr_b16 v[158:159], v10 offset:37376\n"
"s_waitcnt vmcnt(7)\n"
"v_mfma_f32_16x16x32_bf16 v[60:63], v[148:151], v[108:111], v[60:63]\n"
"ds_read_b128 v[128:131], v7 offset:4096\n"
"s_add_u32 m0, s81, 62464\n"
"s_nop 0\n"
"buffer_load_dwordx4 v12, s[40:43], s38 offen lds\n"
"v_mfma_f32_16x16x32_bf16 v[56:59], v[148:151], v[104:107], v[56:59]\n"
"ds_read_b64_tr_b16 v[160:161], v10 offset:37888\n"
"ds_read_b64_tr_b16 v[232:233], v248\n"
"v_mfma_f32_16x16x32_bf16 v[40:43], v[144:147], v[112:115], v[40:43]\n"
"ds_read_b64_tr_b16 v[162:163], v10 offset:38400\n"
"ds_read_b64_tr_b16 v[234:235], v248 offset:512\n"
"v_mfma_f32_16x16x32_bf16 v[64:67], v[148:151], v[112:115], v[64:67]\n"
"ds_read_b128 v[124:127], v7\n"
"s_add_u32 m0, s81, 63488\n"
"s_nop 0\n"
"buffer_load_dwordx4 v13, s[40:43], s38 offen lds\n"
"v_mfma_f32_16x16x32_bf16 v[80:83], v[104:107], v[152:155], v[80:83]\n"
"ds_read_b128 v[132:135], v7 offset:8192\n"
"ds_read_b64_tr_b16 v[236:237], v248 offset:64\n"
"v_mfma_f32_16x16x32_bf16 v[84:87], v[108:111], v[152:155], v[84:87]\n"
"ds_read_b64_tr_b16 v[164:165], v10 offset:38912\n"
"ds_read_b64_tr_b16 v[238:239], v248 offset:576\n"
"v_mfma_f32_16x16x32_bf16 v[88:91], v[112:115], v[152:155], v[88:91]\n"
"ds_read_b64_tr_b16 v[166:167], v10 offset:39424\n"
"s_add_u32 s78, s78, s79\n"
"v_mfma_f32_16x16x32_bf16 v[44:47], v[144:147], v[116:119], v[44:47]\n"
"ds_read_b128 v[136:139], v7 offset:12288\n"
"s_add_u32 m0, s81, 64512\n"
"s_nop 0\n"
"buffer_load_dwordx4 v14, s[40:43], s38 offen lds\n"
"v_mfma_f32_16x16x32_bf16 v[68:71], v[148:151], v[116:119], v[68:71]\n"
"ds_read_b128 v[140:143], v7 offset:16384\n"
"s_lshl_b32 s78, s78, 2\n"
"v_mfma_f32_16x16x32_bf16 v[92:95], v[116:119], v[152:155], v[92:95]\n"
"ds_read_b128 v[188:191], v193 offset:40960\n"
"s_cmp_eq_u32 s68, 2\n"
"s_cselect_b64 exec, -1, 0\n"
"v_xor_b32 v206, s72, v206\n"
"v_xor_b32 v207, s73, v207\n"
"v_cmp_lt_u64 vcc, 3, v[206:207]\n"
"s_mov_b64 exec, -1\n"
"v_mfma_f32_16x16x32_bf16 v[48:51], v[144:147], v[120:123], v[48:51]\n"
"s_nop 2\n"
"s_cbranch_vccz .Lpd2\n"
"s_mov_b32 s83, 50000\n"
".Lpl3:\n"
"buffer_load_dwordx2 v[206:207], v212, s[48:51], 0 offen sc1\n"
"s_waitcnt vmcnt(0)\n"
"v_xor_b32 v206, s72, v206\n"
"v_xor_b32 v207, s73, v207\n"
"v_cmp_lt_u64 vcc, 3, v[206:207]\n"
"s_sub_u32 s83, s83, 1\n"
"s_cbranch_scc1 .Lpd2\n"
"s_nop 2\n"
"s_cbranch_vccz .Lpd2\n"
"s_sleep 4\n"
"s_branch .Lpl3\n"
".Lpd2:\n"
"v_cmp_eq_u32 vcc, 1, v206\n"
"s_add_u32 m0, s81, 65536\n"
"s_nop 0\n"
"buffer_load_dwordx4 v15, s[40:43], s38 offen lds\n"
"v_mfma_f32_16x16x32_bf16 v[72:75], v[148:151], v[120:123], v[72:75]\n"
"s_bcnt1_i32_b64 s98, vcc\n"
"v_cmp_eq_u32 vcc, 2, v206\n"
"v_mfma_f32_16x16x32_bf16 v[96:99], v[120:123], v[152:155], v[96:99]\n"
"s_nop 0\n"
"s_bcnt1_i32_b64 s99, vcc\n"
"s_cmp_gt_u32 s99, s98\n"
"s_cselect_b32 s98, 8192, 0\n"
"v_mfma_f32_16x16x32_bf16 v[172:175], v[104:107], v[104:107], v[172:175]\n"
"v_mov_b32 v206, s98\n"
"v_mov_b32 v207, 256\n"
"v_mfma_f32_16x16x32_bf16 v[176:179], v[32:35], v[184:187], v[176:179]\n"
"s_cmp_eq_u32 s68, 2\n"
"s_cselect_b64 exec, -1, 0\n"
"ds_write_b32 v207, v206\n"
"s_mov_b64 exec, -1\n"
"v_mfma_f32_16x16x32_bf16 v[180:183], v[184:187], v[184:187], v[180:183]\n"
"v_lshrrev_b32 v18, 5, v0\n"
"v_add_u32 v18, s78, v18\n"
"s_waitcnt lgkmcnt(1)\n"
"s_waitcnt lgkmcnt(0)\n"
"s_barrier\n"
"v_mfma_f32_16x16x32_bf16 v[52:55], v[156:159], v[32:35], v[52:55]\n"
"ds_read_b32 v206, v207\n"
"ds_read_b64_tr_b16 v[144:145], v10 offset:49152\n"
"s_add_u32 m0, s82, 24576\n"
"s_nop 0\n"
"buffer_load_dwordx4 v16, s[44:47], s101 offen lds\n"
"v_mfma_f32_16x16x32_bf16 v[36:39], v[156:159], v[128:131], v[36:39]\n"
"ds_read_b64_tr_b16 v[146:147], v10 offset:49664\n"
"buffer_load_dwordx4 v16, s[44:47], s101 offen offset:1024 lds\n"
"v_mfma_f32_16x16x32_bf16 v[60:63], v[160:163], v[128:131], v[60:63]\n"
"ds_read_b128 v[108:111], v8 offset:4096\n"
"v_xor_b32 v18, v18, v201\n"
"v_mfma_f32_16x16x32_bf16 v[56:59], v[160:163], v[124:127], v[56:59]\n"
"ds_read_b64_tr_b16 v[148:149], v10 offset:50176\n"
"v_and_b32 v19, 31, v0\n"
"v_mfma_f32_16x16x32_bf16 v[40:43], v[156:159], v[132:135], v[40:43]\n"
"ds_read_b64_tr_b16 v[150:151], v10 offset:50688\n"
"buffer_load_dwordx4 v16, s[44:47], s101 offen offset:2048 lds\n"
"v_mfma_f32_16x16x32_bf16 v[64:67], v[160:163], v[132:135], v[64:67]\n"
"ds_read_b128 v[104:107], v8\n"
"v_lshlrev_b32 v19, 8, v19\n"
"v_mfma_f32_16x16x32_bf16 v[80:83], v[124:127], v[164:167], v[80:83]\n"
"ds_read_b128 v[112:115], v8 offset:8192\n"
"s_waitcnt lgkmcnt(7)\n"
"v_readfirstlane_b32 s98, v206\n"
"s_nop 1\n"
"s_add_u32 s19, s19, s98\n"
"s_add_u32 s57, s57, s98\n"
"s_add_u32 s58, s58, s98\n"
"s_add_u32 s29, s29, s98\n"
"s_add_u32 s30, s30, s98\n"
"s_add_u32 s18, s18, s98\n"
"s_add_u32 s39, s39, s98\n"
"s_mov_b32 exec_lo, 0\n"
"v_add_u32 v208, s98, v208\n"
"s_mov_b32 exec_lo, -1\n"
"v_mfma_f32_16x16x32_bf16 v[84:87], v[128:131], v[164:167], v[84:87]\n"
"ds_read_b64_tr_b16 v[152:153], v10 offset:51200\n"
"s_add_u32 m0, s82, 27648\n"
"s_nop 0\n"
"buffer_load_dwordx4 v17, s[44:47], s101 offen lds\n"
"v_mfma_f32_16x16x32_bf16 v[88:91], v[132:135], v[164:167], v[88:91]\n"
"ds_read_b64_tr_b16 v[154:155], v10 offset:51712\n"
"v_add_u32 v19, s77, v19\n"
"v_mfma_f32_16x16x32_bf16 v[44:47], v[156:159], v[136:139], v[44:47]\n"
"ds_read_b128 v[116:119], v8 offset:12288\n"
"buffer_load_dwordx4 v17, s[44:47], s101 offen offset:1024 lds\n"
"v_mfma_f32_16x16x32_bf16 v[68:71], v[160:163], v[136:139], v[68:71]\n"
"ds_read_b128 v[120:123], v8 offset:16384\n"
"v_lshl_add_u32 v20, v18, 4, v19\n"
"v_mfma_f32_16x16x32_bf16 v[92:95], v[136:139], v[164:167], v[92:95]\n"
"ds_read_b128 v[184:187], v194 offset:40960\n"
"buffer_store_dwordx4 v[232:235], v1, s[48:51], s24 offen sc1\n"
"v_mfma_f32_16x16x32_bf16 v[48:51], v[156:159], v[140:143], v[48:51]\n"
"v_xor_b32 v18, 2, v18\n"
"v_lshl_add_u32 v24, v18, 4, v19\n"
"v_mfma_f32_16x16x32_bf16 v[72:75], v[160:163], v[140:143], v[72:75]\n"
"v_and_b32 v18, 1, v201\n"
"v_cmp_eq_u32 s[84:85], 1, v18\n"
"v_mfma_f32_16x16x32_bf16 v[96:99], v[140:143], v[164:167], v[96:99]\n"
"v_lshl_add_u32 v18, v202, 2, v18\n"
"v_cmp_le_u32 s[88:89], v18, v201\n"
"v_mfma_f32_16x16x32_bf16 v[172:175], v[124:127], v[124:127], v[172:175]\n"
"v_add_u32 v18, 2, v18\n"
"v_cmp_le_u32 s[90:91], v18, v201\n"
"v_mfma_f32_16x16x32_bf16 v[176:179], v[32:35], v[188:191], v[176:179]\n"
"buffer_load_dwordx4 v17, s[44:47], s101 offen offset:2048 lds\n"
"v_and_b32 v18, 2, v201\n"
"v_mfma_f32_16x16x32_bf16 v[180:183], v[188:191], v[188:191], v[180:183]\n"
"v_cmp_eq_u32 s[86:87], 2, v18\n"
"v_lshrrev_b32 v18, 2, v201\n"
"s_waitcnt lgkmcnt(0)\n"
"v_mfma_f32_16x16x32_bf16 v[52:55], v[144:147], v[32:35], v[52:55]\n"
"ds_read_b64_tr_b16 v[156:157], v10 offset:61440\n"
"v_mfma_f32_16x16x32_bf16 v[36:39], v[144:147], v[108:111], v[36:39]\n"
"ds_read_b64_tr_b16 v[158:159], v10 offset:61952\n"
"v_cmp_eq_u32 s[96:97], v18, v202\n"
"v_mfma_f32_16x16x32_bf16 v[60:63], v[148:151], v[108:111], v[60:63]\n"
"ds_read_b128 v[128:131], v9 offset:4096\n"
"v_mov_b32 v196, 0x0c0c0100\n"
"v_mfma_f32_16x16x32_bf16 v[56:59], v[148:151], v[104:107], v[56:59]\n"
"ds_read_b64_tr_b16 v[160:161], v10 offset:62464\n"
"v_mov_b32 v197, 0x01000c0c\n"
"v_mfma_f32_16x16x32_bf16 v[40:43], v[144:147], v[112:115], v[40:43]\n"
"ds_read_b64_tr_b16 v[162:163], v10 offset:62976\n"
"v_cndmask_b32 v18, v196, v197, s[84:85]\n"
"v_mfma_f32_16x16x32_bf16 v[64:67], v[148:151], v[112:115], v[64:67]\n"
"ds_read_b128 v[124:127], v9\n"
"v_mov_b32 v196, 0x0c0c0302\n"
"v_mfma_f32_16x16x32_bf16 v[80:83], v[104:107], v[152:155], v[80:83]\n"
"ds_read_b128 v[132:135], v9 offset:8192\n"
"buffer_store_dwordx4 v[236:239], v1, s[48:51], s25 offen sc1\n"
"v_mfma_f32_16x16x32_bf16 v[84:87], v[108:111], v[152:155], v[84:87]\n"
"ds_read_b64_tr_b16 v[164:165], v10 offset:63488\n"
"v_mov_b32 v197, 0x03020c0c\n"
"v_mfma_f32_16x16x32_bf16 v[88:91], v[112:115], v[152:155], v[88:91]\n"
"ds_read_b64_tr_b16 v[166:167], v10 offset:64000\n"
"v_cndmask_b32 v19, v196, v197, s[84:85]\n"
"v_mfma_f32_16x16x32_bf16 v[44:47], v[144:147], v[116:119], v[44:47]\n"
"ds_read_b128 v[136:139], v9 offset:12288\n"
"s_cmp_eq_u32 s75, 0\n"
"s_cselect_b64 s[92:93], -1, 0\n"
"v_mfma_f32_16x16x32_bf16 v[68:71], v[148:151], v[116:119], v[68:71]\n"
"ds_read_b128 v[140:143], v9 offset:16384\n"
"s_andn2_b64 s[94:95], s[92:93], s[90:91]\n"
"v_mfma_f32_16x16x32_bf16 v[92:95], v[116:119], v[152:155], v[92:95]\n"
"ds_read_b128 v[188:191], v195 offset:40960\n"
"s_andn2_b64 s[92:93], s[92:93], s[88:89]\n"
"v_mfma_f32_16x16x32_bf16 v[48:51], v[144:147], v[120:123], v[48:51]\n"
"v_mov_b32 v200, 0x0c0c0c0c\n"
"v_cndmask_b32 v196, v18, v200, s[88:89]\n"
"v_mfma_f32_16x16x32_bf16 v[72:75], v[148:151], v[120:123], v[72:75]\n"
"v_cndmask_b32 v198, v200, v18, s[88:89]\n"
"v_cndmask_b32 v197, v19, v200, s[90:91]\n"
"v_mfma_f32_16x16x32_bf16 v[96:99], v[120:123], v[152:155], v[96:99]\n"
"v_cndmask_b32 v199, v200, v19, s[90:91]\n"
"v_mov_b32 v200, 0xf149f2ca\n"
"v_mfma_f32_16x16x32_bf16 v[172:175], v[104:107], v[104:107], v[172:175]\n"
"v_mfma_f32_16x16x32_bf16 v[176:179], v[32:35], v[184:187], v[176:179]\n"
"v_mfma_f32_16x16x32_bf16 v[180:183], v[184:187], v[184:187], v[180:183]\n"
"s_waitcnt lgkmcnt(0)\n"
"s_waitcnt vmcnt(13)\n"
"s_waitcnt vmcnt(8)\n"
"s_barrier\n"
"v_mfma_f32_16x16x32_bf16 v[52:55], v[156:159], v[32:35], v[52:55]\n"
"ds_read_b64_tr_b16 v[144:145], v10\n"
"v_mfma_f32_16x16x32_bf16 v[36:39], v[156:159], v[128:131], v[36:39]\n"
"ds_read_b64_tr_b16 v[146:147], v10 offset:512\n"
"buffer_store_dwordx4 a[224:227], v1, s[48:51], s57 offen sc1\n"
"v_mfma_f32_16x16x32_bf16 v[60:63], v[160:163], v[128:131], v[60:63]\n"
"ds_read_b128 v[108:111], v6 offset:24576\n"
"v_mfma_f32_16x16x32_bf16 v[56:59], v[160:163], v[124:127], v[56:59]\n"
"ds_read_b64_tr_b16 v[148:149], v10 offset:1024\n"
"v_mfma_f32_16x16x32_bf16 v[40:43], v[156:159], v[132:135], v[40:43]\n"
"ds_read_b64_tr_b16 v[150:151], v10 offset:1536\n"
"v_mfma_f32_16x16x32_bf16 v[64:67], v[160:163], v[132:135], v[64:67]\n"
"ds_read_b128 v[104:107], v6 offset:20480\n"
"v_mfma_f32_16x16x32_bf16 v[80:83], v[124:127], v[164:167], v[80:83]\n"
"ds_read_b128 v[112:115], v6 offset:28672\n"
"v_mfma_f32_16x16x32_bf16 v[84:87], v[128:131], v[164:167], v[84:87]\n"
"ds_read_b64_tr_b16 v[152:153], v10 offset:2048\n"
"v_mfma_f32_16x16x32_bf16 v[88:91], v[132:135], v[164:167], v[88:91]\n"
"ds_read_b64_tr_b16 v[154:155], v10 offset:2560\n"
"v_mfma_f32_16x16x32_bf16 v[44:47], v[156:159], v[136:139], v[44:47]\n"
"ds_read_b128 v[116:119], v6 offset:32768\n"
"v_mfma_f32_16x16x32_bf16 v[68:71], v[160:163], v[136:139], v[68:71]\n"
"ds_read_b128 v[120:123], v6 offset:36864\n"
"v_mfma_f32_16x16x32_bf16 v[92:95], v[136:139], v[164:167], v[92:95]\n"
"ds_read_b128 v[216:219], v6 offset:20480\n"
"v_mfma_f32_16x16x32_bf16 v[48:51], v[156:159], v[140:143], v[48:51]\n"
"ds_read_b128 v[220:223], v192 offset:61440\n"
"v_mfma_f32_16x16x32_bf16 v[72:75], v[160:163], v[140:143], v[72:75]\n"
"ds_read_b128 v[224:227], v7 offset:20480\n"
"v_mfma_f32_16x16x32_bf16 v[96:99], v[140:143], v[164:167], v[96:99]\n"
"ds_read_b128 v[228:231], v193 offset:61440\n"
"buffer_store_dwordx4 a[224:227], v1, s[48:51], s58 offen sc1\n"
"v_mfma_f32_16x16x32_bf16 v[172:175], v[124:127], v[124:127], v[172:175]\n"
"ds_read_b128 v[232:235], v8 offset:20480\n"
"v_mfma_f32_16x16x32_bf16 v[176:179], v[32:35], v[188:191], v[176:179]\n"
"ds_read_b128 v[236:239], v194 offset:61440\n"
"v_mfma_f32_16x16x32_bf16 v[180:183], v[188:191], v[188:191], v[180:183]\n"
"ds_read_b128 v[240:243], v9 offset:20480\n"
"ds_read_b128 v[244:247], v195 offset:61440\n"
"s_waitcnt lgkmcnt(8)\n"
"v_mfma_f32_16x16x32_bf16 v[52:55], v[144:147], v[32:35], v[52:55]\n"
"ds_read_b64_tr_b16 v[156:157], v10 offset:12288\n"
"v_mfma_f32_16x16x32_bf16 v[36:39], v[144:147], v[108:111], v[36:39]\n"
"ds_read_b64_tr_b16 v[158:159], v10 offset:12800\n"
"v_mfma_f32_16x16x32_bf16 v[60:63], v[148:151], v[108:111], v[60:63]\n"
"ds_read_b128 v[128:131], v7 offset:24576\n"
"v_mfma_f32_16x16x32_bf16 v[56:59], v[148:151], v[104:107], v[56:59]\n"
"ds_read_b64_tr_b16 v[160:161], v10 offset:13312\n"
"v_mfma_f32_16x16x32_bf16 v[40:43], v[144:147], v[112:115], v[40:43]\n"
"ds_read_b64_tr_b16 v[162:163], v10 offset:13824\n"
"s_cmp_eq_u32 s68, 2\n"
"s_cselect_b64 exec, -1, 0\n"
"buffer_load_dwordx2 v[206:207], v253, s[48:51], 0 offen sc1\n"
"s_mov_b64 exec, -1\n"
"v_mfma_f32_16x16x32_bf16 v[64:67], v[148:151], v[112:115], v[64:67]\n"
"ds_read_b128 v[124:127], v7 offset:20480\n"
"v_mfma_f32_16x16x32_bf16 v[80:83], v[104:107], v[152:155], v[80:83]\n"
"ds_read_b128 v[132:135], v7 offset:28672\n"
"v_mfma_f32_16x16x32_bf16 v[84:87], v[108:111], v[152:155], v[84:87]\n"
"ds_read_b64_tr_b16 v[164:165], v10 offset:14336\n"
"s_waitcnt lgkmcnt(15)\n"
"v_mfma_f32_16x16x32_bf16 v[172:175], v[216:219], v[216:219], v[172:175]\n"
"ds_read_b64_tr_b16 v[166:167], v10 offset:14848\n"
"buffer_store_dwordx4 a[224:227], v1, s[48:51], s29 offen sc1\n"
"s_waitcnt lgkmcnt(15)\n"
"v_mfma_f32_16x16x32_bf16 v[176:179], v[32:35], v[220:223], v[176:179]\n"
"ds_read_b128 v[136:139], v7 offset:32768\n"
"v_mfma_f32_16x16x32_bf16 v[180:183], v[220:223], v[220:223], v[180:183]\n"
"ds_read_b128 v[140:143], v7 offset:36864\n"
"s_waitcnt lgkmcnt(15)\n"
"v_mfma_f32_16x16x32_bf16 v[172:175], v[224:227], v[224:227], v[172:175]\n"
"ds_read_b128 v[20:23], v20\n"
"v_mfma_f32_16x16x32_bf16 v[176:179], v[32:35], v[228:231], v[176:179]\n"
"ds_read_b128 v[24:27], v24\n"
"v_mfma_f32_16x16x32_bf16 v[180:183], v[228:231], v[228:231], v[180:183]\n"
"s_waitcnt lgkmcnt(15)\n"
"v_mfma_f32_16x16x32_bf16 v[172:175], v[232:235], v[232:235], v[172:175]\n"
"v_mfma_f32_16x16x32_bf16 v[176:179], v[32:35], v[236:239], v[176:179]\n"
"v_mfma_f32_16x16x32_bf16 v[180:183], v[236:239], v[236:239], v[180:183]\n"
"s_waitcnt lgkmcnt(14)\n"
"v_mfma_f32_16x16x32_bf16 v[172:175], v[240:243], v[240:243], v[172:175]\n"
"s_waitcnt lgkmcnt(13)\n"
"v_mfma_f32_16x16x32_bf16 v[176:179], v[32:35], v[244:247], v[176:179]\n"
"v_mfma_f32_16x16x32_bf16 v[180:183], v[244:247], v[244:247], v[180:183]\n"
"v_mfma_f32_16x16x32_bf16 v[88:91], v[112:115], v[152:155], v[88:91]\n"
"v_mfma_f32_16x16x32_bf16 v[44:47], v[144:147], v[116:119], v[44:47]\n"
"buffer_store_dwordx4 a[224:227], v1, s[48:51], s29 offen offset:1024 sc1\n"
"v_mfma_f32_16x16x32_bf16 v[68:71], v[148:151], v[116:119], v[68:71]\n"
"v_mfma_f32_16x16x32_bf16 v[92:95], v[116:119], v[152:155], v[92:95]\n"
"v_mfma_f32_16x16x32_bf16 v[48:51], v[144:147], v[120:123], v[48:51]\n"
"v_mfma_f32_16x16x32_bf16 v[72:75], v[148:151], v[120:123], v[72:75]\n"
"s_cmp_eq_u32 s68, 2\n"
"s_cselect_b64 exec, -1, 0\n"
"s_waitcnt vmcnt(2)\n"
"v_cmp_ne_u64 vcc, v[206:207], s[72:73]\n"
"s_mov_b64 exec, -1\n"
"v_mfma_f32_16x16x32_bf16 v[96:99], v[120:123], v[152:155], v[96:99]\n"
"s_waitcnt lgkmcnt(2)\n"
"s_nop 1\n"
"s_cbranch_vccz .Lpd4\n"
"s_mov_b32 s83, 50000\n"
".Lpl5:\n"
"buffer_load_dwordx2 v[206:207], v253, s[48:51], 0 offen sc1\n"
"s_waitcnt vmcnt(0)\n"
"v_cmp_ne_u64 vcc, v[206:207], s[72:73]\n"
"s_sub_u32 s83, s83, 1\n"
"s_cbranch_scc1 .Lpd4\n"
"s_nop 2\n"
"s_cbranch_vccz .Lpd4\n"
"s_sleep 4\n"
"s_branch .Lpl5\n"
".Lpd4:\n"
"s_barrier\n"
"v_mfma_f32_16x16x32_bf16 v[52:55], v[156:159], v[32:35], v[52:55]\n"
"ds_read_b64_tr_b16 v[144:145], v10 offset:24576\n"
"s_cmp_lg_u32 s68, 1\n"
"s_cbranch_scc1 .Lpub6\n"
"s_mov_b64 exec, 1\n"
"buffer_store_dwordx2 v[28:29], off, s[48:51], s27 offset:2048 sc1\n"
"s_mov_b64 exec, -1\n"
".Lpub6:\n"
"buffer_load_dwordx4 a[0:3], v1, s[48:51], s32 offen sc1\n"
"v_mfma_f32_16x16x32_bf16 v[36:39], v[156:159], v[128:131], v[36:39]\n"
"ds_read_b64_tr_b16 v[146:147], v10 offset:25088\n"
"buffer_load_dwordx4 a[32:35], v1, s[48:51], s34 offen sc1\n"
"v_mfma_f32_16x16x32_bf16 v[60:63], v[160:163], v[128:131], v[60:63]\n"
"ds_read_b128 v[108:111], v8 offset:24576\n"
"v_mov_b32 v168, v172\n"
"v_mfma_f32_16x16x32_bf16 v[56:59], v[160:163], v[124:127], v[56:59]\n"
"ds_read_b64_tr_b16 v[148:149], v10 offset:25600\n"
"v_mov_b32 v169, v172\n"
"v_mfma_f32_16x16x32_bf16 v[40:43], v[156:159], v[132:135], v[40:43]\n"
"ds_read_b64_tr_b16 v[150:151], v10 offset:26112\n"
"buffer_load_dwordx4 a[4:7], v1, s[48:51], s32 offen offset:1024 sc1\n"
"v_mfma_f32_16x16x32_bf16 v[64:67], v[160:163], v[132:135], v[64:67]\n"
"ds_read_b128 v[104:107], v8 offset:20480\n"
"buffer_store_dword a224, v209, s[48:51], s30 offen sc1\n"
"v_mfma_f32_16x16x32_bf16 v[80:83], v[124:127], v[164:167], v[80:83]\n"
"ds_read_b128 v[112:115], v8 offset:28672\n"
"v_permlane16_swap_b32 v168, v169\n"
"v_mfma_f32_16x16x32_bf16 v[84:87], v[128:131], v[164:167], v[84:87]\n"
"ds_read_b64_tr_b16 v[152:153], v10 offset:26624\n"
"v_mov_b32 v169, v168\n"
"v_mfma_f32_16x16x32_bf16 v[88:91], v[132:135], v[164:167], v[88:91]\n"
"ds_read_b64_tr_b16 v[154:155], v10 offset:27136\n"
"buffer_load_dwordx4 a[36:39], v1, s[48:51], s34 offen offset:1024 sc1\n"
"v_mfma_f32_16x16x32_bf16 v[44:47], v[156:159], v[136:139], v[44:47]\n"
"ds_read_b128 v[116:119], v8 offset:32768\n"
"v_permlane32_swap_b32 v168, v169\n"
"v_mfma_f32_16x16x32_bf16 v[68:71], v[160:163], v[136:139], v[68:71]\n"
"ds_read_b128 v[120:123], v8 offset:36864\n"
"v_cndmask_b32 v172, v172, v173, s[84:85]\n"
"v_mfma_f32_16x16x32_bf16 v[92:95], v[136:139], v[164:167], v[92:95]\n"
"v_cndmask_b32 v180, v180, v181, s[84:85]\n"
"v_cndmask_b32 v174, v174, v175, s[84:85]\n"
"v_mfma_f32_16x16x32_bf16 v[48:51], v[156:159], v[140:143], v[48:51]\n"
"buffer_load_dwordx4 a[8:11], v1, s[48:51], s32 offen offset:2048 sc1\n"
"v_cndmask_b32 v182, v182, v183, s[84:85]\n"
"v_mfma_f32_16x16x32_bf16 v[72:75], v[160:163], v[140:143], v[72:75]\n"
"v_cndmask_b32 v172, v172, v174, s[86:87]\n"
"v_cndmask_b32 v180, v180, v182, s[86:87]\n"
"v_mfma_f32_16x16x32_bf16 v[96:99], v[140:143], v[164:167], v[96:99]\n"
"v_mul_f32 v169, 0xbb000000, v168\n"
"v_mul_f32 v177, 0xbb000000, v176\n"
"s_waitcnt lgkmcnt(0)\n"
"v_mfma_f32_16x16x32_bf16 v[52:55], v[144:147], v[32:35], v[52:55]\n"
"ds_read_b64_tr_b16 v[156:157], v10 offset:36864\n"
"buffer_load_dwordx4 a[40:43], v1, s[48:51], s34 offen offset:2048 sc1\n"
"v_mfma_f32_16x16x32_bf16 v[36:39], v[144:147], v[108:111], v[36:39]\n"
"ds_read_b64_tr_b16 v[158:159], v10 offset:37376\n"
"v_mul_f32 v172, 0x3b000000, v172\n"
"v_mfma_f32_16x16x32_bf16 v[60:63], v[148:151], v[108:111], v[60:63]\n"
"ds_read_b128 v[128:131], v9 offset:24576\n"
"v_mul_f32 v180, 0x3b000000, v180\n"
"v_mfma_f32_16x16x32_bf16 v[56:59], v[148:151], v[104:107], v[56:59]\n"
"ds_read_b64_tr_b16 v[160:161], v10 offset:37888\n"
"v_fma_f32 v172, -v169, v169, v172\n"
"v_mfma_f32_16x16x32_bf16 v[40:43], v[144:147], v[112:115], v[40:43]\n"
"ds_read_b64_tr_b16 v[162:163], v10 offset:38400\n"
"buffer_load_dwordx4 a[12:15], v1, s[48:51], s32 offen offset:3072 sc1\n"
"v_mfma_f32_16x16x32_bf16 v[64:67], v[148:151], v[112:115], v[64:67]\n"
"ds_read_b128 v[124:127], v9 offset:20480\n"
"v_fma_f32 v180, -v177, v177, v180\n"
"v_mfma_f32_16x16x32_bf16 v[80:83], v[104:107], v[152:155], v[80:83]\n"
"ds_read_b128 v[132:135], v9 offset:28672\n"
"v_max_f32 v172, 0, v172\n"
"v_mfma_f32_16x16x32_bf16 v[84:87], v[108:111], v[152:155], v[84:87]\n"
"ds_read_b64_tr_b16 v[164:165], v10 offset:38912\n"
"buffer_load_dwordx4 a[44:47], v1, s[48:51], s34 offen offset:3072 sc1\n"
"v_mfma_f32_16x16x32_bf16 v[88:91], v[112:115], v[152:155], v[88:91]\n"
"ds_read_b64_tr_b16 v[166:167], v10 offset:39424\n"
"v_max_f32 v180, 0, v180\n"
"v_mfma_f32_16x16x32_bf16 v[44:47], v[144:147], v[116:119], v[44:47]\n"
"ds_read_b128 v[136:139], v9 offset:32768\n"
"v_add_f32 v172, 0x3727c5ac, v172\n"
"v_mfma_f32_16x16x32_bf16 v[68:71], v[148:151], v[116:119], v[68:71]\n"
"ds_read_b128 v[140:143], v9 offset:36864\n"
"v_add_f32 v180, 0x3727c5ac, v180\n"
"v_mfma_f32_16x16x32_bf16 v[92:95], v[116:119], v[152:155], v[92:95]\n"
"buffer_load_dwordx4 a[16:19], v1, s[48:51], s33 offen sc1\n"
"v_rsq_f32 v168, v172\n"
"v_mfma_f32_16x16x32_bf16 v[48:51], v[144:147], v[120:123], v[48:51]\n"
"v_rsq_f32 v176, v180\n"
"v_mfma_f32_16x16x32_bf16 v[72:75], v[148:151], v[120:123], v[72:75]\n"
"s_mov_b64 exec, s[96:97]\n"
"ds_write2_b32 v203, v168, v169 offset1:80\n"
"ds_write2_b32 v204, v176, v177 offset1:80\n"
"s_mov_b64 exec, -1\n"
"v_mfma_f32_16x16x32_bf16 v[96:99], v[120:123], v[152:155], v[96:99]\n"
"buffer_load_dwordx4 a[48:51], v1, s[48:51], s35 offen sc1\n"
"s_waitcnt lgkmcnt(0)\n"
"s_barrier\n"
"v_mfma_f32_16x16x32_bf16 v[52:55], v[156:159], v[32:35], v[52:55]\n"
"ds_read2_b32 v[184:185], v203 offset0:0 offset1:80\n"
"v_mfma_f32_16x16x32_bf16 v[36:39], v[156:159], v[128:131], v[36:39]\n"
"ds_read2_b32 v[186:187], v203 offset0:16 offset1:96\n"
"v_mfma_f32_16x16x32_bf16 v[60:63], v[160:163], v[128:131], v[60:63]\n"
"ds_read2_b32 v[188:189], v203 offset0:32 offset1:112\n"
"buffer_load_dwordx4 a[20:23], v1, s[48:51], s33 offen offset:1024 sc1\n"
"v_mfma_f32_16x16x32_bf16 v[56:59], v[160:163], v[124:127], v[56:59]\n"
"ds_read2_b32 v[190:191], v203 offset0:48 offset1:128\n"
"v_mfma_f32_16x16x32_bf16 v[40:43], v[156:159], v[132:135], v[40:43]\n"
"ds_read2_b32 v[192:193], v203 offset0:64 offset1:144\n"
"v_mfma_f32_16x16x32_bf16 v[64:67], v[160:163], v[132:135], v[64:67]\n"
"ds_read_b128 v[236:239], v205 offset:320\n"
"buffer_load_dwordx4 a[52:55], v1, s[48:51], s35 offen offset:1024 sc1\n"
"v_mfma_f32_16x16x32_bf16 v[80:83], v[124:127], v[164:167], v[80:83]\n"
"ds_read_b128 v[216:219], v205\n"
"v_mfma_f32_16x16x32_bf16 v[84:87], v[128:131], v[164:167], v[84:87]\n"
"ds_read_b128 v[240:243], v205 offset:384\n"
"v_mfma_f32_16x16x32_bf16 v[88:91], v[132:135], v[164:167], v[88:91]\n"
"ds_read_b128 v[220:223], v205 offset:64\n"
"v_mfma_f32_16x16x32_bf16 v[44:47], v[156:159], v[136:139], v[44:47]\n"
"ds_read_b128 v[244:247], v205 offset:448\n"
"buffer_load_dwordx4 a[24:27], v1, s[48:51], s33 offen offset:2048 sc1\n"
"v_mfma_f32_16x16x32_bf16 v[68:71], v[160:163], v[136:139], v[68:71]\n"
"ds_read_b128 v[224:227], v205 offset:128\n"
"v_mfma_f32_16x16x32_bf16 v[92:95], v[136:139], v[164:167], v[92:95]\n"
"ds_read_b128 v[248:251], v205 offset:512\n"
"v_mfma_f32_16x16x32_bf16 v[48:51], v[156:159], v[140:143], v[48:51]\n"
"ds_read_b128 v[228:231], v205 offset:192\n"
"buffer_load_dwordx4 a[56:59], v1, s[48:51], s35 offen offset:2048 sc1\n"
"v_mfma_f32_16x16x32_bf16 v[72:75], v[160:163], v[140:143], v[72:75]\n"
"ds_read_b128 v[252:255], v205 offset:576\n"
"v_mfma_f32_16x16x32_bf16 v[96:99], v[140:143], v[164:167], v[96:99]\n"
"ds_read_b128 v[232:235], v205 offset:256\n"
"v_mov_b32_dpp v76, v56 row_newbcast:0 row_mask:0xf bank_mask:0xf\n"
"buffer_load_dwordx4 a[28:31], v1, s[48:51], s33 offen offset:3072 sc1\n"
"v_mov_b32_dpp v77, v57 row_newbcast:0 row_mask:0xf bank_mask:0xf\n"
"v_mov_b32_dpp v78, v58 row_newbcast:0 row_mask:0xf bank_mask:0xf\n"
"v_mov_b32_dpp v79, v59 row_newbcast:0 row_mask:0xf bank_mask:0xf\n"
"s_waitcnt lgkmcnt(13)\n"
"v_mul_f32 v210, 0x3eb8aa3b, v186\n"
"s_waitcnt lgkmcnt(12)\n"
"v_mul_f32 v211, 0x3eb8aa3b, v188\n"
"s_waitcnt lgkmcnt(11)\n"
"v_mul_f32 v212, 0x3eb8aa3b, v190\n"
"s_waitcnt lgkmcnt(10)\n"
"v_mul_f32 v213, 0x3eb8aa3b, v192\n"
"v_pk_fma_f32 v[56:57], v[76:77], v[184:185], v[56:57] op_sel:[0,1,0] op_sel_hi:[1,1,1]\n"
"v_pk_fma_f32 v[58:59], v[78:79], v[184:185], v[58:59] op_sel:[0,1,0] op_sel_hi:[1,1,1]\n"
"v_pk_mul_f32 v[56:57], v[56:57], v[184:185] op_sel:[0,0] op_sel_hi:[1,0]\n"
"buffer_load_dwordx4 a[60:63], v1, s[48:51], s35 offen offset:3072 sc1\n"
"v_pk_mul_f32 v[58:59], v[58:59], v[184:185] op_sel:[0,0] op_sel_hi:[1,0]\n"
"v_cvt_pk_bf16_f32 v112, v56, v57\n"
"v_cvt_pk_bf16_f32 v113, v58, v59\n"
"v_pk_fma_f32 v[60:61], v[76:77], v[186:187], v[60:61] op_sel:[0,1,0] op_sel_hi:[1,1,1]\n"
"v_pk_fma_f32 v[62:63], v[78:79], v[186:187], v[62:63] op_sel:[0,1,0] op_sel_hi:[1,1,1]\n"
"v_pk_mul_f32 v[60:61], v[60:61], v[186:187] op_sel:[0,0] op_sel_hi:[1,0]\n"
"v_pk_mul_f32 v[62:63], v[62:63], v[186:187] op_sel:[0,0] op_sel_hi:[1,0]\n"
"v_cvt_pk_bf16_f32 v114, v60, v61\n"
"v_cvt_pk_bf16_f32 v115, v62, v63\n"
"v_pk_fma_f32 v[36:37], v[52:53], v[186:187], v[36:37] op_sel:[0,1,0] op_sel_hi:[1,1,1]\n"
"s_add_u32 m0, s61, 0\n"
"s_add_u32 s101, s59, 0\n"
"buffer_load_dwordx4 v1, s[48:51], s101 offen sc1 lds\n"
"v_pk_fma_f32 v[38:39], v[54:55], v[186:187], v[38:39] op_sel:[0,1,0] op_sel_hi:[1,1,1]\n"
"v_pk_mul_f32 v[36:37], v[36:37], v[210:211] op_sel:[0,0] op_sel_hi:[1,0]\n"
"v_pk_mul_f32 v[38:39], v[38:39], v[210:211] op_sel:[0,0] op_sel_hi:[1,0]\n"
"v_cvt_pk_bf16_f32 v104, v36, v37\n"
"v_cvt_pk_bf16_f32 v105, v38, v39\n"
"v_pk_fma_f32 v[64:65], v[76:77], v[188:189], v[64:65] op_sel:[0,1,0] op_sel_hi:[1,1,1]\n"
"v_pk_fma_f32 v[66:67], v[78:79], v[188:189], v[66:67] op_sel:[0,1,0] op_sel_hi:[1,1,1]\n"
"v_pk_mul_f32 v[64:65], v[64:65], v[188:189] op_sel:[0,0] op_sel_hi:[1,0]\n"
"buffer_load_dwordx4 v1, s[48:51], s101 offen offset:1024 sc1 lds\n"
"v_pk_mul_f32 v[66:67], v[66:67], v[188:189] op_sel:[0,0] op_sel_hi:[1,0]\n"
"v_cvt_pk_bf16_f32 v116, v64, v65\n"
"v_cvt_pk_bf16_f32 v117, v66, v67\n"
"v_pk_fma_f32 v[40:41], v[52:53], v[188:189], v[40:41] op_sel:[0,1,0] op_sel_hi:[1,1,1]\n"
"v_pk_fma_f32 v[42:43], v[54:55], v[188:189], v[42:43] op_sel:[0,1,0] op_sel_hi:[1,1,1]\n"
"v_pk_mul_f32 v[40:41], v[40:41], v[210:211] op_sel:[0,1] op_sel_hi:[1,1]\n"
"v_pk_mul_f32 v[42:43], v[42:43], v[210:211] op_sel:[0,1] op_sel_hi:[1,1]\n"
"v_cvt_pk_bf16_f32 v106, v40, v41\n"
"v_cvt_pk_bf16_f32 v107, v42, v43\n"
"v_pk_fma_f32 v[68:69], v[76:77], v[190:191], v[68:69] op_sel:[0,1,0] op_sel_hi:[1,1,1]\n"
"buffer_load_dwordx4 v1, s[48:51], s101 offen offset:2048 sc1 lds\n"
"v_pk_fma_f32 v[70:71], v[78:79], v[190:191], v[70:71] op_sel:[0,1,0] op_sel_hi:[1,1,1]\n"
"v_pk_mul_f32 v[68:69], v[68:69], v[190:191] op_sel:[0,0] op_sel_hi:[1,0]\n"
"v_pk_mul_f32 v[70:71], v[70:71], v[190:191] op_sel:[0,0] op_sel_hi:[1,0]\n"
"v_cvt_pk_bf16_f32 v118, v68, v69\n"
"v_cvt_pk_bf16_f32 v119, v70, v71\n"
"v_pk_fma_f32 v[44:45], v[52:53], v[190:191], v[44:45] op_sel:[0,1,0] op_sel_hi:[1,1,1]\n"
"v_pk_fma_f32 v[46:47], v[54:55], v[190:191], v[46:47] op_sel:[0,1,0] op_sel_hi:[1,1,1]\n"
"v_pk_mul_f32 v[44:45], v[44:45], v[212:213] op_sel:[0,0] op_sel_hi:[1,0]\n"
"v_pk_mul_f32 v[46:47], v[46:47], v[212:213] op_sel:[0,0] op_sel_hi:[1,0]\n"
"v_cvt_pk_bf16_f32 v108, v44, v45\n"
"buffer_load_dwordx4 v1, s[48:51], s101 offen offset:3072 sc1 lds\n"
"v_cvt_pk_bf16_f32 v109, v46, v47\n"
"v_pk_fma_f32 v[72:73], v[76:77], v[192:193], v[72:73] op_sel:[0,1,0] op_sel_hi:[1,1,1]\n"
"v_pk_fma_f32 v[74:75], v[78:79], v[192:193], v[74:75] op_sel:[0,1,0] op_sel_hi:[1,1,1]\n"
"v_pk_mul_f32 v[72:73], v[72:73], v[192:193] op_sel:[0,0] op_sel_hi:[1,0]\n"
"v_pk_mul_f32 v[74:75], v[74:75], v[192:193] op_sel:[0,0] op_sel_hi:[1,0]\n"
"v_cvt_pk_bf16_f32 v120, v72, v73\n"
"v_cvt_pk_bf16_f32 v121, v74, v75\n"
"v_pk_fma_f32 v[48:49], v[52:53], v[192:193], v[48:49] op_sel:[0,1,0] op_sel_hi:[1,1,1]\n"
"v_pk_fma_f32 v[50:51], v[54:55], v[192:193], v[50:51] op_sel:[0,1,0] op_sel_hi:[1,1,1]\n"
"v_pk_mul_f32 v[48:49], v[48:49], v[212:213] op_sel:[0,1] op_sel_hi:[1,1]\n"
"s_add_u32 m0, s61, 32768\n"
"s_add_u32 s101, s59, 32768\n"
"buffer_load_dwordx4 v1, s[48:51], s101 offen sc1 lds\n"
"v_pk_mul_f32 v[50:51], v[50:51], v[212:213] op_sel:[0,1] op_sel_hi:[1,1]\n"
"v_cvt_pk_bf16_f32 v110, v48, v49\n"
"v_cvt_pk_bf16_f32 v111, v50, v51\n"
"v_mov_b32 v100, v80\n"
"v_mov_b32 v101, v80\n"
"s_nop 1\n"
"v_permlane16_swap_b32 v100, v101\n"
"v_mov_b32 v101, v100\n"
"s_nop 1\n"
"v_permlane32_swap_b32 v100, v101\n"
"buffer_load_dwordx4 v1, s[48:51], s101 offen offset:1024 sc1 lds\n"
"v_mov_b32 v101, v100\n"
"v_mfma_f32_16x16x16_bf16 v[132:135], v[112:113], v[104:105], 0\n"
"v_mfma_f32_16x16x16_bf16 v[136:139], v[114:115], v[104:105], 0\n"
"v_mfma_f32_16x16x16_bf16 v[140:143], v[114:115], v[106:107], 0\n"
"v_mfma_f32_16x16x16_bf16 v[144:147], v[116:117], v[106:107], 0\n"
"v_mfma_f32_16x16x16_bf16 v[148:151], v[116:117], v[108:109], 0\n"
"v_mfma_f32_16x16x16_bf16 v[152:155], v[118:119], v[108:109], 0\n"
"v_mfma_f32_16x16x16_bf16 v[156:159], v[118:119], v[110:111], 0\n"
"v_mfma_f32_16x16x16_bf16 v[160:163], v[120:121], v[110:111], 0\n"
"s_waitcnt lgkmcnt(9)\n"
"v_pk_fma_f32 v[80:81], v[100:101], v[236:237], v[80:81]\n"
"buffer_load_dwordx4 v1, s[48:51], s101 offen offset:2048 sc1 lds\n"
"v_pk_fma_f32 v[82:83], v[100:101], v[238:239], v[82:83]\n"
"s_waitcnt lgkmcnt(8)\n"
"v_pk_mul_f32 v[80:81], v[80:81], v[216:217]\n"
"v_pk_mul_f32 v[82:83], v[82:83], v[218:219]\n"
"v_cvt_pk_bf16_f32 v122, v80, v81\n"
"v_cvt_pk_bf16_f32 v123, v82, v83\n"
"s_waitcnt lgkmcnt(7)\n"
"v_pk_fma_f32 v[84:85], v[100:101], v[240:241], v[84:85]\n"
"v_pk_fma_f32 v[86:87], v[100:101], v[242:243], v[86:87]\n"
"s_waitcnt lgkmcnt(6)\n"
"v_pk_mul_f32 v[84:85], v[84:85], v[220:221]\n"
"v_pk_mul_f32 v[86:87], v[86:87], v[222:223]\n"
"v_cvt_pk_bf16_f32 v124, v84, v85\n"
"buffer_load_dwordx4 v1, s[48:51], s101 offen offset:3072 sc1 lds\n"
"v_cvt_pk_bf16_f32 v125, v86, v87\n"
"s_waitcnt lgkmcnt(5)\n"
"v_pk_fma_f32 v[88:89], v[100:101], v[244:245], v[88:89]\n"
"v_pk_fma_f32 v[90:91], v[100:101], v[246:247], v[90:91]\n"
"s_waitcnt lgkmcnt(4)\n"
"v_pk_mul_f32 v[88:89], v[88:89], v[224:225]\n"
"v_pk_mul_f32 v[90:91], v[90:91], v[226:227]\n"
"v_cvt_pk_bf16_f32 v126, v88, v89\n"
"v_cvt_pk_bf16_f32 v127, v90, v91\n"
"s_waitcnt lgkmcnt(3)\n"
"v_pk_fma_f32 v[92:93], v[100:101], v[248:249], v[92:93]\n"
"v_pk_fma_f32 v[94:95], v[100:101], v[250:251], v[94:95]\n"
"s_waitcnt lgkmcnt(2)\n"
"v_pk_mul_f32 v[92:93], v[92:93], v[228:229]\n"
"s_add_u32 m0, s61, 16384\n"
"s_add_u32 s101, s60, 0\n"
"buffer_load_dwordx4 v1, s[48:51], s101 offen sc1 lds\n"
"v_pk_mul_f32 v[94:95], v[94:95], v[230:231]\n"
"v_cvt_pk_bf16_f32 v128, v92, v93\n"
"v_cvt_pk_bf16_f32 v129, v94, v95\n"
"s_waitcnt vmcnt(22)\n"
"s_barrier\n"
"s_cmp_lg_u32 s68, 1\n"
"s_cbranch_scc1 .Lpub7\n"
"s_mov_b64 exec, 1\n"
"buffer_store_dwordx2 v[28:29], off, s[48:51], s18 offset:256 sc1\n"
"s_mov_b64 exec, -1\n"
".Lpub7:\n"
"s_waitcnt lgkmcnt(1)\n"
"v_pk_fma_f32 v[96:97], v[100:101], v[252:253], v[96:97]\n"
"buffer_load_dwordx4 v1, s[48:51], s101 offen offset:1024 sc1 lds\n"
"v_pk_fma_f32 v[98:99], v[100:101], v[254:255], v[98:99]\n"
"s_waitcnt lgkmcnt(0)\n"
"v_pk_mul_f32 v[96:97], v[96:97], v[232:233]\n"
"v_pk_mul_f32 v[98:99], v[98:99], v[234:235]\n"
"v_cvt_pk_bf16_f32 v130, v96, v97\n"
"v_cvt_pk_bf16_f32 v131, v98, v99\n"
"v_cndmask_b32 v132, v132, v133, s[84:85]\n"
"v_cndmask_b32 v134, v134, v135, s[84:85]\n"
"v_cndmask_b32 v136, v136, v137, s[84:85]\n"
"v_cndmask_b32 v138, v138, v139, s[84:85]\n"
"v_cndmask_b32 v36, v132, v136, s[88:89]\n"
"buffer_load_dwordx4 v1, s[48:51], s101 offen offset:2048 sc1 lds\n"
"v_cndmask_b32 v37, v134, v138, s[90:91]\n"
"v_cndmask_b32 v140, v140, v141, s[84:85]\n"
"v_cndmask_b32 v142, v142, v143, s[84:85]\n"
"v_cndmask_b32 v144, v144, v145, s[84:85]\n"
"v_cndmask_b32 v146, v146, v147, s[84:85]\n"
"v_cndmask_b32 v38, v140, v144, s[88:89]\n"
"v_cndmask_b32 v39, v142, v146, s[90:91]\n"
"v_cndmask_b32 v148, v148, v149, s[84:85]\n"
"v_cndmask_b32 v150, v150, v151, s[84:85]\n"
"v_cndmask_b32 v152, v152, v153, s[84:85]\n"
"buffer_load_dwordx4 v1, s[48:51], s101 offen offset:3072 sc1 lds\n"
"v_cndmask_b32 v154, v154, v155, s[84:85]\n"
"v_cndmask_b32 v40, v148, v152, s[88:89]\n"
"v_cndmask_b32 v41, v150, v154, s[90:91]\n"
"v_cndmask_b32 v156, v156, v157, s[84:85]\n"
"v_cndmask_b32 v158, v158, v159, s[84:85]\n"
"v_cndmask_b32 v160, v160, v161, s[84:85]\n"
"v_cndmask_b32 v162, v162, v163, s[84:85]\n"
"v_cndmask_b32 v42, v156, v160, s[88:89]\n"
"v_cndmask_b32 v43, v158, v162, s[90:91]\n"
"v_cndmask_b32 v36, v36, v200, s[92:93]\n"
"s_add_u32 m0, s61, 49152\n"
"s_add_u32 s101, s60, 32768\n"
"buffer_load_dwordx4 v1, s[48:51], s101 offen sc1 lds\n"
"v_cndmask_b32 v37, v37, v200, s[94:95]\n"
"v_max_f32 v44, v36, v37\n"
"v_max_f32 v45, v38, v39\n"
"v_max_f32 v46, v40, v41\n"
"v_max_f32 v47, v42, v43\n"
"v_permlane16_swap_b32 v44, v45\n"
"s_nop 0\n"
"v_permlane16_swap_b32 v46, v47\n"
"v_max_f32 v44, v44, v45\n"
"buffer_load_dwordx4 v1, s[48:51], s101 offen offset:1024 sc1 lds\n"
"v_max_f32 v46, v46, v47\n"
"s_nop 1\n"
"v_permlane32_swap_b32 v44, v46\n"
"v_max_f32 v44, v44, v46\n"
"v_mov_b32 v46, v44\n"
"s_nop 1\n"
"v_permlane32_swap_b32 v44, v46\n"
"v_mov_b32 v45, v44\n"
"v_mov_b32 v47, v46\n"
"s_nop 0\n"
"v_permlane16_swap_b32 v44, v45\n"
"v_permlane16_swap_b32 v46, v47\n"
"v_sub_f32 v36, v36, v44\n"
"v_lshlrev_b32 v18, 10, v202\n"
"v_lshl_add_u32 v18, v201, 4, v18\n"
"buffer_load_dwordx4 v[168:171], v18, s[48:51], s56 offen sc1\n"
"v_sub_f32 v37, v37, v44\n"
"v_sub_f32 v38, v38, v45\n"
"v_sub_f32 v39, v39, v45\n"
"v_sub_f32 v40, v40, v46\n"
"v_sub_f32 v41, v41, v46\n"
"v_sub_f32 v42, v42, v47\n"
"v_sub_f32 v43, v43, v47\n"
"v_exp_f32 v36, v36\n"
"s_add_u32 s56, s56, 4096\n"
"buffer_load_dwordx4 v[172:175], v18, s[48:51], s56 offen sc1\n"
"v_exp_f32 v37, v37\n"
"v_exp_f32 v38, v38\n"
"v_exp_f32 v39, v39\n"
"v_exp_f32 v40, v40\n"
"s_add_u32 s56, s56, 4096\n"
"buffer_load_dwordx4 v[176:179], v18, s[48:51], s56 offen sc1\n"
"v_exp_f32 v41, v41\n"
"v_exp_f32 v42, v42\n"
"v_exp_f32 v43, v43\n"
"v_add_f32 v44, v36, v37\n"
"v_add_f32 v45, v38, v39\n"
"v_add_f32 v46, v40, v41\n"
"s_add_u32 s56, s56, 4096\n"
"buffer_load_dwordx4 v[180:183], v18, s[48:51], s56 offen sc1\n"
"v_add_f32 v47, v42, v43\n"
"v_permlane16_swap_b32 v44, v45\n"
"s_nop 0\n"
"v_permlane16_swap_b32 v46, v47\n"
"v_add_f32 v44, v44, v45\n"
"v_add_f32 v46, v46, v47\n"
"s_nop 1\n"
"v_permlane32_swap_b32 v44, v46\n"
"v_add_f32 v44, v44, v46\n"
"v_rcp_f32 v44, v44\n"
"s_nop 0\n"
"v_mov_b32 v46, v44\n"
"s_nop 1\n"
"v_permlane32_swap_b32 v44, v46\n"
"v_mov_b32 v45, v44\n"
"v_mov_b32 v47, v46\n"
"s_nop 0\n"
"v_permlane16_swap_b32 v44, v45\n"
"v_permlane16_swap_b32 v46, v47\n"
"v_mul_f32 v36, v36, v44\n"
"v_mul_f32 v37, v37, v44\n"
"v_cvt_pk_bf16_f32 v36, v36, v37\n"
"v_mul_f32 v38, v38, v45\n"
"v_mul_f32 v39, v39, v45\n"
"v_cvt_pk_bf16_f32 v38, v38, v39\n"
"v_mul_f32 v40, v40, v46\n"
"v_mul_f32 v41, v41, v46\n"
"v_cvt_pk_bf16_f32 v40, v40, v41\n"
"v_mul_f32 v42, v42, v47\n"
"v_mul_f32 v43, v43, v47\n"
"v_cvt_pk_bf16_f32 v42, v42, v43\n"
"v_perm_b32 v132, v36, v36, v196\n"
"v_perm_b32 v133, v36, v36, v197\n"
"v_perm_b32 v134, v36, v36, v198\n"
"v_perm_b32 v135, v36, v36, v199\n"
"v_perm_b32 v136, v38, v38, v196\n"
"v_perm_b32 v137, v38, v38, v197\n"
"v_perm_b32 v138, v38, v38, v198\n"
"v_perm_b32 v139, v38, v38, v199\n"
"v_mfma_f32_16x16x32_bf16 v[148:151], v[122:125], v[132:135], 0\n"
"v_perm_b32 v140, v40, v40, v196\n"
"v_perm_b32 v141, v40, v40, v197\n"
"v_perm_b32 v142, v40, v40, v198\n"
"s_cmp_eq_u32 s68, 2\n"
"s_cselect_b64 exec, -1, 0\n"
"buffer_load_dwordx2 v[206:207], v208, s[48:51], 0 offen offset:2048 sc1\n"
"s_mov_b64 exec, -1\n"
"v_perm_b32 v143, v40, v40, v199\n"
"v_mfma_f32_16x16x32_bf16 v[152:155], v[124:127], v[136:139], 0\n"
"v_perm_b32 v144, v42, v42, v196\n"
"v_perm_b32 v145, v42, v42, v197\n"
"v_perm_b32 v146, v42, v42, v198\n"
"v_perm_b32 v147, v42, v42, v199\n"
"v_mfma_f32_16x16x32_bf16 v[156:159], v[126:129], v[140:143], 0\n"
"s_nop 0\n"
"v_mfma_f32_16x16x32_bf16 v[160:163], v[128:131], v[144:147], 0\n"
"v_cvt_pk_bf16_f32 v48, v148, v149\n"
"v_cvt_pk_bf16_f32 v50, v152, v153\n"
"v_cvt_pk_bf16_f32 v49, v150, v151\n"
"v_cvt_pk_bf16_f32 v51, v154, v155\n"
"s_nop 1\n"
"v_cvt_pk_bf16_f32 v52, v156, v157\n"
"s_nop 0\n"
"v_cvt_pk_bf16_f32 v54, v160, v161\n"
"v_cvt_pk_bf16_f32 v53, v158, v159\n"
"v_cvt_pk_bf16_f32 v55, v162, v163\n"
"v_permlane16_swap_b32 v48, v50\n"
"v_permlane16_swap_b32 v49, v51\n"
"v_permlane16_swap_b32 v52, v54\n"
"v_permlane16_swap_b32 v53, v55\n"
"buffer_store_dwordx4 v[48:51], v1, s[48:51], s57 offen sc1\n"
"buffer_store_dwordx4 v[52:55], v1, s[48:51], s58 offen sc1\n"
"s_lshl_b32 s77, s74, 1\n"
"buffer_load_dwordx4 v1, s[48:51], s101 offen offset:2048 sc1 lds\n"
"s_add_u32 s77, s77, s31\n"
"s_lshl_b32 s77, s77, 2\n"
"s_add_u32 s77, s77, s68\n"
"s_lshl_b32 s77, s77, 14\n"
"buffer_load_dwordx4 v1, s[48:51], s101 offen offset:3072 sc1 lds\n"
"s_xor_b32 s78, s77, 65536\n"
"s_add_u32 s20, s77, s19\n"
"s_add_u32 s21, s20, 4096\n"
"s_add_u32 s22, s78, s19\n"
"s_add_u32 s23, s22, 4096\n"
"s_and_b32 s77, s68, 1\n"
"s_lshl_b32 s78, s71, 1\n"
"s_add_u32 m0, s61, 65536\n"
"s_add_u32 s101, s59, 65536\n"
"buffer_load_dwordx4 v1, s[48:51], s101 offen sc1 lds\n"
"s_add_u32 s79, s77, s78\n"
"s_xor_b32 s80, s79, 1\n"
"s_mul_i32 s77, s68, 28672\n"
"s_add_u32 s77, s77, 1572864\n"
"s_mul_i32 s79, s79, 114688\n"
"buffer_load_dwordx4 v1, s[48:51], s101 offen offset:1024 sc1 lds\n"
"s_mul_i32 s80, s80, 114688\n"
"s_add_u32 s79, s77, s79\n"
"s_add_u32 s80, s77, s80\n"
"s_add_u32 s77, 0, 0\n"
"s_and_b32 s77, s77, 3\n"
"s_mul_i32 s77, s77, 7168\n"
"s_add_u32 s40, s79, s77\n"
"buffer_load_dwordx4 v1, s[48:51], s101 offen offset:2048 sc1 lds\n"
"s_add_u32 s41, s80, s77\n"
"s_add_u32 s77, 0, 1\n"
"s_and_b32 s77, s77, 3\n"
"s_mul_i32 s77, s77, 7168\n"
"s_add_u32 s42, s79, s77\n"
"s_add_u32 s43, s80, s77\n"
"s_add_u32 s77, 0, 2\n"
"buffer_load_dwordx4 v1, s[48:51], s101 offen offset:3072 sc1 lds\n"
"s_and_b32 s77, s77, 3\n"
"s_mul_i32 s77, s77, 7168\n"
"s_add_u32 s44, s79, s77\n"
"s_add_u32 s45, s80, s77\n"
"s_add_u32 s77, 0, 3\n"
"s_and_b32 s77, s77, 3\n"
"s_mul_i32 s77, s77, 7168\n"
"s_add_u32 m0, s61, 81920\n"
"s_add_u32 s101, s60, 65536\n"
"buffer_load_dwordx4 v1, s[48:51], s101 offen sc1 lds\n"
"s_add_u32 s46, s79, s77\n"
"s_add_u32 s47, s80, s77\n"
"s_xor_b32 s77, s68, 1\n"
"s_mul_i32 s77, s77, 12288\n"
"s_add_u32 s77, s77, 98304\n"
"buffer_load_dwordx4 v1, s[48:51], s101 offen offset:1024 sc1 lds\n"
"v_add_u32 v2, s77, v1\n"
"s_xor_b32 s77, s68, 2\n"
"s_mul_i32 s77, s77, 12288\n"
"s_add_u32 s77, s77, 102400\n"
"v_add_u32 v3, s77, v1\n"
"s_xor_b32 s77, s68, 3\n"
"s_mul_i32 s77, s77, 12288\n"
"s_add_u32 s77, s77, 106496\n"
"v_add_u32 v4, s77, v1\n"
"s_mul_i32 s77, s68, 12288\n"
"s_add_u32 s77, s77, 98304\n"
"v_add_u32 v5, s77, v1\n"
"v_mov_b32 v7, v209\n"
"s_lshl_b32 s78, s68, 16\n"
"s_add_u32 s78, s78, s19\n"
"s_add_u32 s78, s78, 262144\n"
"v_add_u32 v10, s78, v1\n"
"s_lshr_b32 s78, s68, 1\n"
"s_lshl_b32 s78, s78, 14\n"
"s_and_b32 s79, s68, 1\n"
"s_lshl_b32 s79, s79, 12\n"
"s_add_u32 s78, s78, s79\n"
"s_add_u32 s78, s78, s19\n"
"s_add_u32 s88, s78, 1572864\n"
"v_add_u32 v12, 65536, v1\n"
"v_add_u32 v13, 131072, v1\n"
"v_lshrrev_b32 v14, 5, v0\n"
"v_lshlrev_b32 v14, 6, v14\n"
"v_add_u32 v14, 157696, v14\n"
"s_add_u32 s85, s19, 1605632\n"
"v_lshlrev_b32 v6, 3, v201\n"
"v_bfe_u32 v18, v0, 5, 1\n"
"v_bfe_u32 v19, v0, 2, 1\n"
"v_lshl_add_u32 v18, v18, 1, v19\n"
"v_bfe_u32 v19, v0, 3, 2\n"
"v_lshl_add_u32 v18, v18, 2, v19\n"
"v_and_b32 v19, 3, v0\n"
"v_lshl_add_u32 v18, v18, 2, v19\n"
"s_lshl_b32 s77, s68, 8\n"
"s_add_u32 s77, s77, 157696\n"
"v_lshl_add_u32 v18, v18, 2, s77\n"
"s_waitcnt vmcnt(12)\n"
"v_add_f32 v168, v168, v176\n"
"v_add_f32 v169, v169, v177\n"
"v_add_f32 v170, v170, v178\n"
"v_add_f32 v171, v171, v179\n"
"s_waitcnt vmcnt(11)\n"
"v_add_f32 v172, v172, v180\n"
"v_add_f32 v173, v173, v181\n"
"v_add_f32 v174, v174, v182\n"
"v_add_f32 v175, v175, v183\n"
"v_add_f32 v168, v168, v172\n"
"v_add_f32 v169, v169, v173\n"
"v_add_f32 v170, v170, v174\n"
"v_add_f32 v171, v171, v175\n"
"v_permlane32_swap_b32 v168, v169\n"
"s_nop 0\n"
"v_permlane32_swap_b32 v170, v171\n"
"v_add_f32 v168, v168, v169\n"
"v_add_f32 v170, v170, v171\n"
"s_nop 1\n"
"v_permlane16_swap_b32 v168, v170\n"
"v_add_f32 v168, v168, v170\n"
"ds_write_b32 v18, v168\n"
"s_cmp_eq_u32 s68, 2\n"
"s_cselect_b64 exec, -1, 0\n"
"s_waitcnt vmcnt(10)\n"
"v_cmp_ne_u64 vcc, v[206:207], s[72:73]\n"
"s_mov_b64 exec, -1\n"
"s_waitcnt vmcnt(8)\n"
"s_nop 2\n"
"s_cbranch_vccz .Lpd8\n"
"s_mov_b32 s83, 50000\n"
".Lpl9:\n"
"buffer_load_dwordx2 v[206:207], v208, s[48:51], 0 offen offset:2048 sc1\n"
"s_waitcnt vmcnt(0)\n"
"v_cmp_ne_u64 vcc, v[206:207], s[72:73]\n"
"s_sub_u32 s83, s83, 1\n"
"s_cbranch_scc1 .Lpd8\n"
"s_nop 2\n"
"s_cbranch_vccz .Lpd8\n"
"s_sleep 4\n"
"s_branch .Lpl9\n"
".Lpd8:\n"
"s_barrier\n"
"buffer_load_dwordx4 v[68:71], v1, s[48:51], s22 offen sc1\n"
"s_nop 13\n"
"buffer_load_dwordx4 v[76:79], v1, s[48:51], s22 offen offset:1024 sc1\n"
"s_nop 13\n"
"buffer_load_dwordx4 v[84:87], v1, s[48:51], s22 offen offset:2048 sc1\n"
"s_nop 13\n"
"buffer_load_dwordx4 v[92:95], v1, s[48:51], s22 offen offset:3072 sc1\n"
"s_nop 13\n"
"buffer_load_dwordx4 v[100:103], v1, s[48:51], s23 offen sc1\n"
"s_nop 13\n"
"buffer_load_dwordx4 v[108:111], v1, s[48:51], s23 offen offset:1024 sc1\n"
"s_nop 13\n"
"buffer_load_dwordx4 v[116:119], v1, s[48:51], s23 offen offset:2048 sc1\n"
"s_nop 13\n"
"buffer_load_dwordx4 v[124:127], v1, s[48:51], s23 offen offset:3072 sc1\n"
"s_nop 13\n"
"buffer_load_dwordx4 v[64:67], v1, s[48:51], s20 offen sc1\n"
"s_nop 13\n"
"buffer_load_dwordx4 v[72:75], v1, s[48:51], s20 offen offset:1024 sc1\n"
"s_waitcnt vmcnt(9)\n"
"v_max3_u32 v36, v68, v69, v70\n"
"s_waitcnt vmcnt(8)\n"
"v_max3_u32 v36, v71, v76, v36\n"
"v_max3_u32 v36, v77, v78, v36\n"
"v_max_u32 v36, v79, v36\n"
"v_cmp_eq_u32 vcc, -1, v36\n"
"s_nop 4\n"
"s_cbranch_vccnz .Lsl10\n"
".Lbk11:\n"
"v_mfma_f32_32x32x16_bf16 v[240:255], a[32:35], v[68:71], 0\n"
"buffer_load_dwordx4 v[80:83], v1, s[48:51], s20 offen offset:2048 sc1\n"
"v_mfma_f32_32x32x16_bf16 v[224:239], a[0:3], v[68:71], 0\n"
"s_waitcnt vmcnt(8)\n"
"v_max3_u32 v36, v84, v85, v86\n"
"s_waitcnt vmcnt(7)\n"
"v_max3_u32 v36, v87, v92, v36\n"
"v_max3_u32 v36, v93, v94, v36\n"
"v_max_u32 v36, v95, v36\n"
"v_cmp_eq_u32 vcc, -1, v36\n"
"buffer_load_dwordx4 v[88:91], v1, s[48:51], s20 offen offset:3072 sc1\n"
"v_mfma_f32_32x32x16_bf16 v[240:255], a[36:39], v[76:79], v[240:255]\n"
"buffer_load_dwordx4 v[96:99], v1, s[48:51], s21 offen sc1\n"
"v_mfma_f32_32x32x16_bf16 v[224:239], a[4:7], v[76:79], v[224:239]\n"
"s_nop 0\n"
"s_cbranch_vccnz .Lsl12\n"
".Lbk13:\n"
"buffer_load_dwordx4 v[104:107], v1, s[48:51], s21 offen offset:1024 sc1\n"
"v_mfma_f32_32x32x16_bf16 v[240:255], a[40:43], v[84:87], v[240:255]\n"
"buffer_load_dwordx4 v[112:115], v1, s[48:51], s21 offen offset:2048 sc1\n"
"v_mfma_f32_32x32x16_bf16 v[224:239], a[8:11], v[84:87], v[224:239]\n"
"s_waitcnt vmcnt(10)\n"
"v_max3_u32 v36, v100, v101, v102\n"
"s_waitcnt vmcnt(9)\n"
"v_max3_u32 v36, v103, v108, v36\n"
"v_max3_u32 v36, v109, v110, v36\n"
"v_max_u32 v36, v111, v36\n"
"v_cmp_eq_u32 vcc, -1, v36\n"
"buffer_load_dwordx4 v[120:123], v1, s[48:51], s21 offen offset:3072 sc1\n"
"v_mfma_f32_32x32x16_bf16 v[240:255], a[44:47], v[92:95], v[240:255]\n"
"buffer_load_dwordx4 v1, s[48:51], s101 offen offset:2048 sc1 lds\n"
"v_mfma_f32_32x32x16_bf16 v[224:239], a[12:15], v[92:95], v[224:239]\n"
"s_nop 0\n"
"s_cbranch_vccnz .Lsl14\n"
".Lbk15:\n"
"v_mfma_f32_32x32x16_bf16 v[240:255], a[48:51], v[100:103], v[240:255]\n"
"buffer_load_dwordx4 v1, s[48:51], s101 offen offset:3072 sc1 lds\n"
"v_mfma_f32_32x32x16_bf16 v[224:239], a[16:19], v[100:103], v[224:239]\n"
"s_waitcnt vmcnt(11)\n"
"v_max3_u32 v36, v116, v117, v118\n"
"s_waitcnt vmcnt(10)\n"
"v_max3_u32 v36, v119, v124, v36\n"
"v_max3_u32 v36, v125, v126, v36\n"
"v_max_u32 v36, v127, v36\n"
"v_cmp_eq_u32 vcc, -1, v36\n"
"v_mfma_f32_32x32x16_bf16 v[240:255], a[52:55], v[108:111], v[240:255]\n"
"buffer_load_dwordx4 a[64:67], v1, s[48:51], s40 offen sc1\n"
"v_mfma_f32_32x32x16_bf16 v[224:239], a[20:23], v[108:111], v[224:239]\n"
"s_nop 1\n"
"s_cbranch_vccnz .Lsl16\n"
".Lbk17:\n"
"v_mfma_f32_32x32x16_bf16 v[240:255], a[56:59], v[116:119], v[240:255]\n"
"buffer_load_dwordx4 a[68:71], v1, s[48:51], s41 offen sc1\n"
"v_mfma_f32_32x32x16_bf16 v[224:239], a[24:27], v[116:119], v[224:239]\n"
"s_waitcnt vmcnt(11)\n"
"v_max3_u32 v36, v64, v65, v66\n"
"s_waitcnt vmcnt(10)\n"
"v_max3_u32 v36, v67, v72, v36\n"
"v_max3_u32 v36, v73, v74, v36\n"
"v_max_u32 v36, v75, v36\n"
"v_cmp_eq_u32 vcc, -1, v36\n"
"v_mfma_f32_32x32x16_bf16 v[240:255], a[60:63], v[124:127], v[240:255]\n"
"buffer_load_dwordx4 a[72:75], v1, s[48:51], s42 offen sc1\n"
"v_mfma_f32_32x32x16_bf16 v[224:239], a[28:31], v[124:127], v[224:239]\n"
"s_nop 1\n"
"s_cbranch_vccnz .Lsl18\n"
".Lbk19:\n"
"v_mfma_f32_32x32x16_bf16 v[208:223], a[32:35], v[64:67], 0\n"
"buffer_load_dwordx4 a[76:79], v1, s[48:51], s43 offen sc1\n"
"v_lshlrev_b32 v176, 16, v20\n"
"v_and_b32 v177, 0xffff0000, v20\n"
"v_lshlrev_b32 v178, 16, v21\n"
"v_and_b32 v179, 0xffff0000, v21\n"
"v_lshlrev_b32 v180, 16, v22\n"
"v_mfma_f32_32x32x16_bf16 v[192:207], a[0:3], v[64:67], 0\n"
"s_waitcnt vmcnt(11)\n"
"v_max3_u32 v36, v80, v81, v82\n"
"s_waitcnt vmcnt(10)\n"
"v_max3_u32 v36, v83, v88, v36\n"
"v_max3_u32 v36, v89, v90, v36\n"
"v_max_u32 v36, v91, v36\n"
"v_cmp_eq_u32 vcc, -1, v36\n"
"v_and_b32 v181, 0xffff0000, v22\n"
"v_mfma_f32_32x32x16_bf16 v[208:223], a[36:39], v[72:75], v[208:223]\n"
"buffer_load_dwordx4 a[80:83], v1, s[48:51], s44 offen sc1\n"
"v_lshlrev_b32 v182, 16, v23\n"
"v_and_b32 v183, 0xffff0000, v23\n"
"v_lshlrev_b32 v184, 16, v24\n"
"v_and_b32 v185, 0xffff0000, v24\n"
"v_lshlrev_b32 v186, 16, v25\n"
"v_mfma_f32_32x32x16_bf16 v[192:207], a[4:7], v[72:75], v[192:207]\n"
"s_cbranch_vccnz .Lsl20\n"
".Lbk21:\n"
"v_and_b32 v187, 0xffff0000, v25\n"
"v_lshlrev_b32 v188, 16, v26\n"
"v_and_b32 v189, 0xffff0000, v26\n"
"v_lshlrev_b32 v190, 16, v27\n"
"v_and_b32 v191, 0xffff0000, v27\n"
"v_mfma_f32_32x32x16_bf16 v[208:223], a[40:43], v[80:83], v[208:223]\n"
"ds_write_b128 v4, v[240:243]\n"
"ds_write_b128 v4, v[244:247] offset:1024\n"
"ds_write_b128 v4, v[248:251] offset:2048\n"
"ds_write_b128 v4, v[252:255] offset:3072\n"
"ds_write_b128 v3, v[224:227]\n"
"ds_write_b128 v3, v[228:231] offset:1024\n"
"v_mfma_f32_32x32x16_bf16 v[192:207], a[8:11], v[80:83], v[192:207]\n"
"s_waitcnt vmcnt(10)\n"
"v_max3_u32 v36, v96, v97, v98\n"
"s_waitcnt vmcnt(9)\n"
"v_max3_u32 v36, v99, v104, v36\n"
"v_max3_u32 v36, v105, v106, v36\n"
"v_max_u32 v36, v107, v36\n"
"v_cmp_eq_u32 vcc, -1, v36\n"
"buffer_load_dwordx4 a[84:87], v1, s[48:51], s45 offen sc1\n"
"v_mfma_f32_32x32x16_bf16 v[208:223], a[44:47], v[88:91], v[208:223]\n"
"ds_write_b128 v3, v[232:235] offset:2048\n"
"ds_write_b128 v3, v[236:239] offset:3072\n"
"v_mfma_f32_32x32x16_bf16 v[192:207], a[12:15], v[88:91], v[192:207]\n"
"s_cbranch_vccnz .Lsl22\n"
".Lbk23:\n"
"buffer_load_dwordx4 a[88:91], v1, s[48:51], s46 offen sc1\n"
"v_mfma_f32_32x32x16_bf16 v[208:223], a[48:51], v[96:99], v[208:223]\n"
"v_mfma_f32_32x32x16_bf16 v[192:207], a[16:19], v[96:99], v[192:207]\n"
"s_waitcnt vmcnt(10)\n"
"v_max3_u32 v36, v112, v113, v114\n"
"s_waitcnt vmcnt(9)\n"
"v_max3_u32 v36, v115, v120, v36\n"
"v_max3_u32 v36, v121, v122, v36\n"
"v_max_u32 v36, v123, v36\n"
"v_cmp_eq_u32 vcc, -1, v36\n"
"buffer_load_dwordx4 a[92:95], v1, s[48:51], s47 offen sc1\n"
"v_mfma_f32_32x32x16_bf16 v[208:223], a[52:55], v[104:107], v[208:223]\n"
"v_mfma_f32_32x32x16_bf16 v[192:207], a[20:23], v[104:107], v[192:207]\n"
"s_nop 1\n"
"s_cbranch_vccnz .Lsl24\n"
".Lbk25:\n"
"buffer_load_dwordx4 a[96:99], v1, s[48:51], s40 offen offset:1024 sc1\n"
"v_mfma_f32_32x32x16_bf16 v[208:223], a[56:59], v[112:115], v[208:223]\n"
"v_mfma_f32_32x32x16_bf16 v[208:223], a[60:63], v[120:123], v[208:223]\n"
"buffer_load_dwordx4 a[100:103], v1, s[48:51], s41 offen offset:1024 sc1\n"
"v_mfma_f32_32x32x16_bf16 v[192:207], a[24:27], v[112:115], v[192:207]\n"
"v_mfma_f32_32x32x16_bf16 v[192:207], a[28:31], v[120:123], v[192:207]\n"
"buffer_load_dwordx4 a[104:107], v1, s[48:51], s42 offen offset:1024 sc1\n"
"s_nop 7\n"
"ds_write_b128 v2, v[208:211]\n"
"ds_write_b128 v2, v[212:215] offset:1024\n"
"ds_write_b128 v2, v[216:219] offset:2048\n"
"ds_write_b128 v2, v[220:223] offset:3072\n"
"s_waitcnt lgkmcnt(0)\n"
"s_barrier\n"
"ds_read_b128 v[128:131], v5\n"
"buffer_load_dwordx4 a[108:111], v1, s[48:51], s43 offen offset:1024 sc1\n"
"ds_read_b128 v[132:135], v5 offset:1024\n"
"ds_read_b128 v[136:139], v5 offset:2048\n"
"ds_read_b128 v[140:143], v5 offset:3072\n"
"buffer_load_dwordx4 a[112:115], v1, s[48:51], s44 offen offset:1024 sc1\n"
"ds_read_b128 v[144:147], v5 offset:4096\n"
"ds_read_b128 v[148:151], v5 offset:5120\n"
"ds_read_b128 v[152:155], v5 offset:6144\n"
"ds_read_b128 v[156:159], v5 offset:7168\n"
"ds_read_b128 v[160:163], v5 offset:8192\n"
"buffer_load_dwordx4 a[116:119], v1, s[48:51], s45 offen offset:1024 sc1\n"
"ds_read_b128 v[164:167], v5 offset:9216\n"
"ds_read_b128 v[168:171], v5 offset:10240\n"
"ds_read_b128 v[172:175], v5 offset:11264\n"
"v_pk_add_f32 v[192:193], v[192:193], v[176:177]\n"
"v_pk_add_f32 v[194:195], v[194:195], v[178:179]\n"
"buffer_load_dwordx4 a[120:123], v1, s[48:51], s46 offen offset:1024 sc1\n"
"v_pk_add_f32 v[196:197], v[196:197], v[180:181]\n"
"v_pk_add_f32 v[198:199], v[198:199], v[182:183]\n"
"v_pk_add_f32 v[200:201], v[200:201], v[184:185]\n"
"v_pk_add_f32 v[202:203], v[202:203], v[186:187]\n"
"v_pk_add_f32 v[204:205], v[204:205], v[188:189]\n"
"buffer_load_dwordx4 a[124:127], v1, s[48:51], s47 offen offset:1024 sc1\n"
"v_pk_add_f32 v[206:207], v[206:207], v[190:191]\n"
"s_waitcnt lgkmcnt(11)\n"
"v_pk_add_f32 v[192:193], v[192:193], v[128:129]\n"
"v_pk_add_f32 v[194:195], v[194:195], v[130:131]\n"
"s_waitcnt lgkmcnt(10)\n"
"v_pk_add_f32 v[196:197], v[196:197], v[132:133]\n"
"v_pk_add_f32 v[198:199], v[198:199], v[134:135]\n"
"buffer_load_dwordx4 a[128:131], v1, s[48:51], s40 offen offset:2048 sc1\n"
"s_waitcnt lgkmcnt(9)\n"
"v_pk_add_f32 v[200:201], v[200:201], v[136:137]\n"
"v_pk_add_f32 v[202:203], v[202:203], v[138:139]\n"
"s_waitcnt lgkmcnt(8)\n"
"v_pk_add_f32 v[204:205], v[204:205], v[140:141]\n"
"v_pk_add_f32 v[206:207], v[206:207], v[142:143]\n"
"s_waitcnt lgkmcnt(7)\n"
"v_pk_add_f32 v[192:193], v[192:193], v[144:145]\n"
"buffer_load_dwordx4 a[132:135], v1, s[48:51], s41 offen offset:2048 sc1\n"
"v_pk_add_f32 v[194:195], v[194:195], v[146:147]\n"
"s_waitcnt lgkmcnt(6)\n"
"v_pk_add_f32 v[196:197], v[196:197], v[148:149]\n"
"v_pk_add_f32 v[198:199], v[198:199], v[150:151]\n"
"s_waitcnt lgkmcnt(5)\n"
"v_pk_add_f32 v[200:201], v[200:201], v[152:153]\n"
"v_pk_add_f32 v[202:203], v[202:203], v[154:155]\n"
"buffer_load_dwordx4 a[136:139], v1, s[48:51], s42 offen offset:2048 sc1\n"
"s_waitcnt lgkmcnt(4)\n"
"v_pk_add_f32 v[204:205], v[204:205], v[156:157]\n"
"v_pk_add_f32 v[206:207], v[206:207], v[158:159]\n"
"s_waitcnt lgkmcnt(3)\n"
"v_pk_add_f32 v[192:193], v[192:193], v[160:161]\n"
"v_pk_add_f32 v[194:195], v[194:195], v[162:163]\n"
"s_waitcnt lgkmcnt(2)\n"
"v_pk_add_f32 v[196:197], v[196:197], v[164:165]\n"
"buffer_load_dwordx4 a[140:143], v1, s[48:51], s43 offen offset:2048 sc1\n"
"v_pk_add_f32 v[198:199], v[198:199], v[166:167]\n"
"s_waitcnt lgkmcnt(1)\n"
"v_pk_add_f32 v[200:201], v[200:201], v[168:169]\n"
"v_pk_add_f32 v[202:203], v[202:203], v[170:171]\n"
"s_waitcnt lgkmcnt(0)\n"
"v_pk_add_f32 v[204:205], v[204:205], v[172:173]\n"
"v_pk_add_f32 v[206:207], v[206:207], v[174:175]\n"
"buffer_load_dwordx4 a[144:147], v1, s[48:51], s44 offen offset:2048 sc1\n"
"v_cvt_pk_bf16_f32 v20, v192, v193\n"
"v_cvt_pk_bf16_f32 v21, v194, v195\n"
"v_cvt_pk_bf16_f32 v22, v196, v197\n"
"v_cvt_pk_bf16_f32 v23, v198, v199\n"
"v_cvt_pk_bf16_f32 v24, v200, v201\n"
"v_cvt_pk_bf16_f32 v25, v202, v203\n"
"v_cvt_pk_bf16_f32 v26, v204, v205\n"
"v_cvt_pk_bf16_f32 v27, v206, v207\n"
"buffer_store_dwordx4 v[20:23], v1, s[48:51], s29 offen sc1\n"
"buffer_store_dwordx4 v[24:27], v1, s[48:51], s29 offen offset:1024 sc1\n"
"v_dot2_f32_bf16 v8, v20, v32, 0\n"
"v_dot2_f32_bf16 v9, v20, v20, 0\n"
"v_dot2_f32_bf16 v8, v21, v32, v8\n"
"v_dot2_f32_bf16 v9, v21, v21, v9\n"
"v_dot2_f32_bf16 v8, v22, v32, v8\n"
"v_dot2_f32_bf16 v9, v22, v22, v9\n"
"v_dot2_f32_bf16 v8, v23, v32, v8\n"
"v_dot2_f32_bf16 v9, v23, v23, v9\n"
"v_dot2_f32_bf16 v8, v24, v32, v8\n"
"v_dot2_f32_bf16 v9, v24, v24, v9\n"
"v_dot2_f32_bf16 v8, v25, v32, v8\n"
"v_dot2_f32_bf16 v9, v25, v25, v9\n"
"v_dot2_f32_bf16 v8, v26, v32, v8\n"
"v_dot2_f32_bf16 v9, v26, v26, v9\n"
"v_dot2_f32_bf16 v8, v27, v32, v8\n"
"v_dot2_f32_bf16 v9, v27, v27, v9\n"
"s_nop 2\n"
"v_permlane32_swap_b32 v8, v9\n"
"v_add_f32 v8, v8, v9\n"
"buffer_store_dword v8, v7, s[48:51], s30 offen sc1\n"
"s_barrier\n"
"s_lshl_b32 s78, s74, 1\n"
"s_add_u32 m0, s61, 98304\n"
"s_add_u32 s101, s59, 98304\n"
"buffer_load_dwordx4 v1, s[48:51], s101 offen sc1 lds\n"
"s_add_u32 s79, s31, s78\n"
"s_xor_b32 s80, s79, 1\n"
"s_lshl_b32 s77, s68, 16\n"
"buffer_load_dwordx4 v1, s[48:51], s101 offen offset:1024 sc1 lds\n"
"s_add_u32 s77, s77, s19\n"
"s_add_u32 s77, s77, 524288\n"
"s_lshl_b32 s79, s79, 18\n"
"s_lshl_b32 s80, s80, 18\n"
"s_add_u32 s79, s77, s79\n"
"buffer_load_dwordx4 v1, s[48:51], s101 offen offset:2048 sc1 lds\n"
"s_add_u32 s80, s77, s80\n"
"s_add_u32 s77, 0, 0\n"
"s_and_b32 s77, s77, 3\n"
"s_lshl_b32 s77, s77, 14\n"
"s_add_u32 s20, s79, s77\n"
"buffer_load_dwordx4 v1, s[48:51], s101 offen offset:3072 sc1 lds\n"
"s_add_u32 s21, s80, s77\n"
"s_add_u32 s77, 0, 1\n"
"s_and_b32 s77, s77, 3\n"
"s_lshl_b32 s77, s77, 14\n"
"s_add_u32 s22, s79, s77\n"
"s_add_u32 m0, s61, 114688\n"
"s_add_u32 s101, s60, 98304\n"
"buffer_load_dwordx4 v1, s[48:51], s101 offen sc1 lds\n"
"s_add_u32 s23, s80, s77\n"
"s_add_u32 s77, 0, 2\n"
"s_and_b32 s77, s77, 3\n"
"buffer_load_dwordx4 v1, s[48:51], s101 offen offset:1024 sc1 lds\n"
"s_lshl_b32 s77, s77, 14\n"
"s_add_u32 s24, s79, s77\n"
"s_add_u32 s25, s80, s77\n"
"s_add_u32 s77, 0, 3\n"
"s_and_b32 s77, s77, 3\n"
"buffer_load_dwordx4 v1, s[48:51], s101 offen offset:2048 sc1 lds\n"
"s_lshl_b32 s77, s77, 14\n"
"s_add_u32 s26, s79, s77\n"
"s_add_u32 s27, s80, s77\n"
"v_mov_b32 v240, -1\n"
"v_mov_b32 v241, -1\n"
"buffer_load_dwordx4 v1, s[48:51], s101 offen offset:3072 sc1 lds\n"
"v_mov_b32 v242, -1\n"
"v_mov_b32 v243, -1\n"
"s_and_b32 s79, s68, 1\n"
"s_lshl_b32 s77, s31, 5\n"
"s_add_u32 s77, s77, s75\n"
"s_lshl_b32 s77, s77, 10\n"
"s_lshl_b32 s78, s71, 7\n"
"s_add_u32 s77, s77, s78\n"
"s_lshl_b32 s78, s79, 6\n"
"s_add_u32 s28, s77, s78\n"
"v_lshrrev_b32 v19, 5, v0\n"
"v_and_b32 v0, 31, v0\n"
"v_lshlrev_b32 v0, 10, v0\n"
"v_lshl_add_u32 v0, v19, 4, v0\n"
"ds_read_b128 v[56:59], v1\n"
"ds_read_b128 v[60:63], v1 offset:32768\n"
"ds_read_b128 v[64:67], v1 offset:1024\n"
"ds_read_b128 v[68:71], v1 offset:33792\n"
"ds_read_b128 v[72:75], v1 offset:2048\n"
"s_waitcnt vmcnt(9)\n"
"buffer_load_dwordx4 v[128:131], v10, s[48:51], s62 offen sc1\n"
"s_nop 13\n"
"buffer_load_dwordx4 v[132:135], v10, s[48:51], s62 offen offset:1024 sc1\n"
"s_nop 13\n"
"buffer_load_dwordx4 v[136:139], v10, s[48:51], s62 offen offset:2048 sc1\n"
"s_nop 13\n"
"buffer_load_dwordx4 v[140:143], v10, s[48:51], s62 offen offset:3072 sc1\n"
"s_nop 13\n"
"s_mov_b32 s62, 4096\n"
"buffer_load_dwordx4 v[144:147], v10, s[48:51], s62 offen sc1\n"
"s_nop 12\n"
"buffer_load_dwordx4 v[148:151], v10, s[48:51], s62 offen offset:1024 sc1\n"
"s_nop 13\n"
"buffer_load_dwordx4 v[152:155], v10, s[48:51], s62 offen offset:2048 sc1\n"
"s_nop 13\n"
"buffer_load_dwordx4 v[156:159], v10, s[48:51], s62 offen offset:3072 sc1\n"
"s_nop 13\n"
"s_mov_b32 s62, 16384\n"
"buffer_load_dwordx4 v[160:163], v10, s[48:51], s62 offen sc1\n"
"s_nop 12\n"
"buffer_load_dwordx4 v[164:167], v10, s[48:51], s62 offen offset:1024 sc1\n"
"s_nop 13\n"
"buffer_load_dwordx4 v[168:171], v10, s[48:51], s62 offen offset:2048 sc1\n"
"s_nop 13\n"
"buffer_load_dwordx4 v[172:175], v10, s[48:51], s62 offen offset:3072 sc1\n"
"s_nop 13\n"
"s_mov_b32 s62, 20480\n"
"buffer_load_dwordx4 v[176:179], v10, s[48:51], s62 offen sc1\n"
"s_nop 12\n"
"buffer_load_dwordx4 v[180:183], v10, s[48:51], s62 offen offset:1024 sc1\n"
"s_nop 13\n"
"buffer_load_dwordx4 v[184:187], v10, s[48:51], s62 offen offset:2048 sc1\n"
"s_nop 13\n"
"buffer_load_dwordx4 v[188:191], v10, s[48:51], s62 offen offset:3072 sc1\n"
"s_waitcnt vmcnt(15)\n"
"v_max3_u32 v8, v128, v129, v130\n"
"s_waitcnt vmcnt(14)\n"
"v_max3_u32 v8, v131, v132, v8\n"
"v_max3_u32 v8, v133, v134, v8\n"
"v_max_u32 v8, v135, v8\n"
"v_cmp_eq_u32 vcc, -1, v8\n"
"s_nop 4\n"
"s_cbranch_vccnz .Lsl26\n"
".Lbk27:\n"
"s_waitcnt lgkmcnt(3)\n"
"v_mfma_f32_32x32x16_bf16 v[80:95], v[56:59], v[128:131], 0\n"
"ds_read_b128 v[76:79], v1 offset:34816\n"
"s_mov_b32 s62, 32768\n"
"buffer_load_dwordx4 v[192:195], v10, s[48:51], s62 offen sc1\n"
"v_mfma_f32_32x32x16_bf16 v[96:111], v[60:63], v[128:131], 0\n"
"ds_read_b128 v[56:59], v1 offset:3072\n"
"s_waitcnt vmcnt(14)\n"
"v_max3_u32 v8, v136, v137, v138\n"
"s_waitcnt vmcnt(13)\n"
"v_max3_u32 v8, v139, v140, v8\n"
"v_max3_u32 v8, v141, v142, v8\n"
"v_max_u32 v8, v143, v8\n"
"v_cmp_eq_u32 vcc, -1, v8\n"
"s_waitcnt lgkmcnt(3)\n"
"v_mfma_f32_32x32x16_bf16 v[80:95], v[64:67], v[132:135], v[80:95]\n"
"ds_read_b128 v[60:63], v1 offset:35840\n"
"buffer_load_dwordx4 v[196:199], v10, s[48:51], s62 offen offset:1024 sc1\n"
"v_mfma_f32_32x32x16_bf16 v[96:111], v[68:71], v[132:135], v[96:111]\n"
"ds_read_b128 v[64:67], v1 offset:4096\n"
"s_cbranch_vccnz .Lsl28\n"
".Lbk29:\n"
"s_waitcnt lgkmcnt(3)\n"
"v_mfma_f32_32x32x16_bf16 v[80:95], v[72:75], v[136:139], v[80:95]\n"
"ds_read_b128 v[68:71], v1 offset:36864\n"
"buffer_load_dwordx4 v[200:203], v10, s[48:51], s62 offen offset:2048 sc1\n"
"v_mfma_f32_32x32x16_bf16 v[96:111], v[76:79], v[136:139], v[96:111]\n"
"ds_read_b128 v[72:75], v1 offset:5120\n"
"s_waitcnt vmcnt(14)\n"
"v_max3_u32 v8, v144, v145, v146\n"
"s_waitcnt vmcnt(13)\n"
"v_max3_u32 v8, v147, v148, v8\n"
"v_max3_u32 v8, v149, v150, v8\n"
"v_max_u32 v8, v151, v8\n"
"v_cmp_eq_u32 vcc, -1, v8\n"
"s_waitcnt lgkmcnt(3)\n"
"v_mfma_f32_32x32x16_bf16 v[80:95], v[56:59], v[140:143], v[80:95]\n"
"ds_read_b128 v[76:79], v1 offset:37888\n"
"buffer_load_dwordx4 v[204:207], v10, s[48:51], s62 offen offset:3072 sc1\n"
"v_mfma_f32_32x32x16_bf16 v[96:111], v[60:63], v[140:143], v[96:111]\n"
"ds_read_b128 v[56:59], v1 offset:6144\n"
"s_cbranch_vccnz .Lsl30\n"
".Lbk31:\n"
"s_waitcnt lgkmcnt(3)\n"
"v_mfma_f32_32x32x16_bf16 v[80:95], v[64:67], v[144:147], v[80:95]\n"
"ds_read_b128 v[60:63], v1 offset:38912\n"
"s_mov_b32 s62, 36864\n"
"buffer_load_dwordx4 v[208:211], v10, s[48:51], s62 offen sc1\n"
"v_mfma_f32_32x32x16_bf16 v[96:111], v[68:71], v[144:147], v[96:111]\n"
"ds_read_b128 v[64:67], v1 offset:7168\n"
"s_waitcnt vmcnt(14)\n"
"v_max3_u32 v8, v152, v153, v154\n"
"s_waitcnt vmcnt(13)\n"
"v_max3_u32 v8, v155, v156, v8\n"
"v_max3_u32 v8, v157, v158, v8\n"
"v_max_u32 v8, v159, v8\n"
"v_cmp_eq_u32 vcc, -1, v8\n"
"s_waitcnt lgkmcnt(3)\n"
"v_mfma_f32_32x32x16_bf16 v[80:95], v[72:75], v[148:151], v[80:95]\n"
"ds_read_b128 v[68:71], v1 offset:39936\n"
"buffer_load_dwordx4 v[212:215], v10, s[48:51], s62 offen offset:1024 sc1\n"
"v_mfma_f32_32x32x16_bf16 v[96:111], v[76:79], v[148:151], v[96:111]\n"
"ds_read_b128 v[72:75], v1 offset:8192\n"
"s_cbranch_vccnz .Lsl32\n"
".Lbk33:\n"
"s_waitcnt lgkmcnt(3)\n"
"v_mfma_f32_32x32x16_bf16 v[80:95], v[56:59], v[152:155], v[80:95]\n"
"ds_read_b128 v[76:79], v1 offset:40960\n"
"buffer_load_dwordx4 v[216:219], v10, s[48:51], s62 offen offset:2048 sc1\n"
"v_mfma_f32_32x32x16_bf16 v[96:111], v[60:63], v[152:155], v[96:111]\n"
"ds_read_b128 v[56:59], v1 offset:9216\n"
"s_waitcnt vmcnt(14)\n"
"v_max3_u32 v8, v160, v161, v162\n"
"s_waitcnt vmcnt(13)\n"
"v_max3_u32 v8, v163, v164, v8\n"
"v_max3_u32 v8, v165, v166, v8\n"
"v_max_u32 v8, v167, v8\n"
"v_cmp_eq_u32 vcc, -1, v8\n"
"s_waitcnt lgkmcnt(3)\n"
"v_mfma_f32_32x32x16_bf16 v[80:95], v[64:67], v[156:159], v[80:95]\n"
"ds_read_b128 v[60:63], v1 offset:41984\n"
"buffer_load_dwordx4 v[220:223], v10, s[48:51], s62 offen offset:3072 sc1\n"
"v_mfma_f32_32x32x16_bf16 v[96:111], v[68:71], v[156:159], v[96:111]\n"
"ds_read_b128 v[64:67], v1 offset:10240\n"
"s_cbranch_vccnz .Lsl34\n"
".Lbk35:\n"
"s_waitcnt lgkmcnt(3)\n"
"v_mfma_f32_32x32x16_bf16 v[80:95], v[72:75], v[160:163], v[80:95]\n"
"ds_read_b128 v[68:71], v1 offset:43008\n"
"s_mov_b32 s62, 49152\n"
"buffer_load_dwordx4 a[224:227], v10, s[48:51], s62 offen sc1\n"
"v_mfma_f32_32x32x16_bf16 v[96:111], v[76:79], v[160:163], v[96:111]\n"
"ds_read_b128 v[72:75], v1 offset:11264\n"
"s_waitcnt vmcnt(14)\n"
"v_max3_u32 v8, v168, v169, v170\n"
"s_waitcnt vmcnt(13)\n"
"v_max3_u32 v8, v171, v172, v8\n"
"v_max3_u32 v8, v173, v174, v8\n"
"v_max_u32 v8, v175, v8\n"
"v_cmp_eq_u32 vcc, -1, v8\n"
"s_waitcnt lgkmcnt(3)\n"
"v_mfma_f32_32x32x16_bf16 v[80:95], v[56:59], v[164:167], v[80:95]\n"
"ds_read_b128 v[76:79], v1 offset:44032\n"
"buffer_load_dwordx4 a[228:231], v10, s[48:51], s62 offen offset:1024 sc1\n"
"v_mfma_f32_32x32x16_bf16 v[96:111], v[60:63], v[164:167], v[96:111]\n"
"ds_read_b128 v[56:59], v1 offset:12288\n"
"s_cbranch_vccnz .Lsl36\n"
".Lbk37:\n"
"s_waitcnt lgkmcnt(3)\n"
"v_mfma_f32_32x32x16_bf16 v[80:95], v[64:67], v[168:171], v[80:95]\n"
"ds_read_b128 v[60:63], v1 offset:45056\n"
"buffer_load_dwordx4 a[232:235], v10, s[48:51], s62 offen offset:2048 sc1\n"
"v_mfma_f32_32x32x16_bf16 v[96:111], v[68:71], v[168:171], v[96:111]\n"
"ds_read_b128 v[64:67], v1 offset:13312\n"
"s_waitcnt vmcnt(14)\n"
"v_max3_u32 v8, v176, v177, v178\n"
"s_waitcnt vmcnt(13)\n"
"v_max3_u32 v8, v179, v180, v8\n"
"v_max3_u32 v8, v181, v182, v8\n"
"v_max_u32 v8, v183, v8\n"
"v_cmp_eq_u32 vcc, -1, v8\n"
"s_waitcnt lgkmcnt(3)\n"
"v_mfma_f32_32x32x16_bf16 v[80:95], v[72:75], v[172:175], v[80:95]\n"
"ds_read_b128 v[68:71], v1 offset:46080\n"
"buffer_load_dwordx4 a[236:239], v10, s[48:51], s62 offen offset:3072 sc1\n"
"v_mfma_f32_32x32x16_bf16 v[96:111], v[76:79], v[172:175], v[96:111]\n"
"ds_read_b128 v[72:75], v1 offset:14336\n"
"s_cbranch_vccnz .Lsl38\n"
".Lbk39:\n"
"s_waitcnt lgkmcnt(3)\n"
"v_mfma_f32_32x32x16_bf16 v[80:95], v[56:59], v[176:179], v[80:95]\n"
"ds_read_b128 v[76:79], v1 offset:47104\n"
"s_mov_b32 s62, 53248\n"
"buffer_load_dwordx4 a[240:243], v10, s[48:51], s62 offen sc1\n"
"v_mfma_f32_32x32x16_bf16 v[96:111], v[60:63], v[176:179], v[96:111]\n"
"ds_read_b128 v[56:59], v1 offset:15360\n"
"s_waitcnt vmcnt(14)\n"
"v_max3_u32 v8, v184, v185, v186\n"
"s_waitcnt vmcnt(13)\n"
"v_max3_u32 v8, v187, v188, v8\n"
"v_max3_u32 v8, v189, v190, v8\n"
"v_max_u32 v8, v191, v8\n"
"v_cmp_eq_u32 vcc, -1, v8\n"
"s_waitcnt lgkmcnt(3)\n"
"v_mfma_f32_32x32x16_bf16 v[80:95], v[64:67], v[180:183], v[80:95]\n"
"ds_read_b128 v[60:63], v1 offset:48128\n"
"buffer_load_dwordx4 a[244:247], v10, s[48:51], s62 offen offset:1024 sc1\n"
"v_mfma_f32_32x32x16_bf16 v[96:111], v[68:71], v[180:183], v[96:111]\n"
"ds_read_b128 v[64:67], v1 offset:16384\n"
"s_cbranch_vccnz .Lsl40\n"
".Lbk41:\n"
"s_waitcnt lgkmcnt(3)\n"
"v_mfma_f32_32x32x16_bf16 v[80:95], v[72:75], v[184:187], v[80:95]\n"
"ds_read_b128 v[68:71], v1 offset:49152\n"
"buffer_load_dwordx4 a[248:251], v10, s[48:51], s62 offen offset:2048 sc1\n"
"v_mfma_f32_32x32x16_bf16 v[96:111], v[76:79], v[184:187], v[96:111]\n"
"ds_read_b128 v[72:75], v1 offset:17408\n"
"s_waitcnt vmcnt(14)\n"
"v_max3_u32 v8, v192, v193, v194\n"
"s_waitcnt vmcnt(13)\n"
"v_max3_u32 v8, v195, v196, v8\n"
"v_max3_u32 v8, v197, v198, v8\n"
"v_max_u32 v8, v199, v8\n"
"v_cmp_eq_u32 vcc, -1, v8\n"
"s_waitcnt lgkmcnt(3)\n"
"v_mfma_f32_32x32x16_bf16 v[80:95], v[56:59], v[188:191], v[80:95]\n"
"ds_read_b128 v[76:79], v1 offset:50176\n"
"buffer_load_dwordx4 a[252:255], v10, s[48:51], s62 offen offset:3072 sc1\n"
"v_mfma_f32_32x32x16_bf16 v[96:111], v[60:63], v[188:191], v[96:111]\n"
"ds_read_b128 v[56:59], v1 offset:18432\n"
"s_cbranch_vccnz .Lsl42\n"
".Lbk43:\n"
"s_waitcnt lgkmcnt(3)\n"
"v_mfma_f32_32x32x16_bf16 v[80:95], v[64:67], v[192:195], v[80:95]\n"
"ds_read_b128 v[60:63], v1 offset:51200\n"
"buffer_load_dwordx4 v[40:43], v1, s[48:51], s88 offen sc1\n"
"v_mfma_f32_32x32x16_bf16 v[96:111], v[68:71], v[192:195], v[96:111]\n"
"ds_read_b128 v[64:67], v1 offset:19456\n"
"s_waitcnt vmcnt(14)\n"
"v_max3_u32 v8, v200, v201, v202\n"
"s_waitcnt vmcnt(13)\n"
"v_max3_u32 v8, v203, v204, v8\n"
"v_max3_u32 v8, v205, v206, v8\n"
"v_max_u32 v8, v207, v8\n"
"v_cmp_eq_u32 vcc, -1, v8\n"
"s_waitcnt lgkmcnt(3)\n"
"v_mfma_f32_32x32x16_bf16 v[80:95], v[72:75], v[196:199], v[80:95]\n"
"ds_read_b128 v[68:71], v1 offset:52224\n"
"buffer_load_dwordx4 v[44:47], v1, s[48:51], s88 offen offset:1024 sc1\n"
"v_mfma_f32_32x32x16_bf16 v[96:111], v[76:79], v[196:199], v[96:111]\n"
"ds_read_b128 v[72:75], v1 offset:20480\n"
"s_cbranch_vccnz .Lsl44\n"
".Lbk45:\n"
"s_waitcnt lgkmcnt(3)\n"
"v_mfma_f32_32x32x16_bf16 v[80:95], v[56:59], v[200:203], v[80:95]\n"
"ds_read_b128 v[76:79], v1 offset:53248\n"
"buffer_load_dwordx4 v[48:51], v1, s[48:51], s88 offen offset:2048 sc1\n"
"v_mfma_f32_32x32x16_bf16 v[96:111], v[60:63], v[200:203], v[96:111]\n"
"ds_read_b128 v[56:59], v1 offset:21504\n"
"s_waitcnt vmcnt(14)\n"
"v_max3_u32 v8, v208, v209, v210\n"
"s_waitcnt vmcnt(13)\n"
"v_max3_u32 v8, v211, v212, v8\n"
"v_max3_u32 v8, v213, v214, v8\n"
"v_max_u32 v8, v215, v8\n"
"v_cmp_eq_u32 vcc, -1, v8\n"
"s_waitcnt lgkmcnt(3)\n"
"v_mfma_f32_32x32x16_bf16 v[80:95], v[64:67], v[204:207], v[80:95]\n"
"ds_read_b128 v[60:63], v1 offset:54272\n"
"buffer_load_dwordx4 v[52:55], v1, s[48:51], s88 offen offset:3072 sc1\n"
"v_mfma_f32_32x32x16_bf16 v[96:111], v[68:71], v[204:207], v[96:111]\n"
"ds_read_b128 v[64:67], v1 offset:22528\n"
"s_cbranch_vccnz .Lsl46\n"
".Lbk47:\n"
"s_waitcnt lgkmcnt(3)\n"
"v_mfma_f32_32x32x16_bf16 v[80:95], v[72:75], v[208:211], v[80:95]\n"
"ds_read_b128 v[68:71], v1 offset:55296\n"
"s_add_u32 m0, s61, 131072\n"
"s_add_u32 s101, s59, 131072\n"
"buffer_load_dwordx4 v1, s[48:51], s101 offen sc1 lds\n"
"v_mfma_f32_32x32x16_bf16 v[96:111], v[76:79], v[208:211], v[96:111]\n"
"ds_read_b128 v[72:75], v1 offset:23552\n"
"s_waitcnt vmcnt(14)\n"
"v_max3_u32 v8, v216, v217, v218\n"
"s_waitcnt vmcnt(13)\n"
"v_max3_u32 v8, v219, v220, v8\n"
"v_max3_u32 v8, v221, v222, v8\n"
"v_max_u32 v8, v223, v8\n"
"v_cmp_eq_u32 vcc, -1, v8\n"
"s_waitcnt lgkmcnt(3)\n"
"v_mfma_f32_32x32x16_bf16 v[80:95], v[56:59], v[212:215], v[80:95]\n"
"ds_read_b128 v[76:79], v1 offset:56320\n"
"buffer_load_dwordx4 v1, s[48:51], s101 offen offset:1024 sc1 lds\n"
"v_mfma_f32_32x32x16_bf16 v[96:111], v[60:63], v[212:215], v[96:111]\n"
"ds_read_b128 v[56:59], v1 offset:24576\n"
"s_cbranch_vccnz .Lsl48\n"
".Lbk49:\n"
"s_waitcnt lgkmcnt(3)\n"
"v_mfma_f32_32x32x16_bf16 v[80:95], v[64:67], v[216:219], v[80:95]\n"
"ds_read_b128 v[60:63], v1 offset:57344\n"
"s_waitcnt vmcnt(13)\n"
"v_accvgpr_read_b32 v18, a224\n"
"v_accvgpr_read_b32 v19, a225\n"
"v_max_u32 v8, v18, v19\n"
"v_accvgpr_read_b32 v18, a226\n"
"v_accvgpr_read_b32 v19, a227\n"
"v_max3_u32 v8, v18, v19, v8\n"
"buffer_load_dwordx4 v1, s[48:51], s101 offen offset:2048 sc1 lds\n"
"v_mfma_f32_32x32x16_bf16 v[96:111], v[68:71], v[216:219], v[96:111]\n"
"ds_read_b128 v[64:67], v1 offset:25600\n"
"s_waitcnt vmcnt(13)\n"
"v_accvgpr_read_b32 v18, a228\n"
"v_accvgpr_read_b32 v19, a229\n"
"v_max3_u32 v8, v18, v19, v8\n"
"v_accvgpr_read_b32 v18, a230\n"
"v_accvgpr_read_b32 v19, a231\n"
"v_max3_u32 v8, v18, v19, v8\n"
"v_cmp_eq_u32 vcc, -1, v8\n"
"s_waitcnt lgkmcnt(3)\n"
"v_mfma_f32_32x32x16_bf16 v[80:95], v[72:75], v[220:223], v[80:95]\n"
"ds_read_b128 v[68:71], v1 offset:58368\n"
"buffer_load_dwordx4 v1, s[48:51], s101 offen offset:3072 sc1 lds\n"
"v_mfma_f32_32x32x16_bf16 v[96:111], v[76:79], v[220:223], v[96:111]\n"
"ds_read_b128 v[72:75], v1 offset:26624\n"
"s_cbranch_vccnz .Lsl50\n"
".Lbk51:\n"
"s_waitcnt lgkmcnt(3)\n"
"v_mfma_f32_32x32x16_bf16 v[80:95], v[56:59], a[224:227], v[80:95]\n"
"ds_read_b128 v[76:79], v1 offset:59392\n"
"s_waitcnt vmcnt(13)\n"
"v_accvgpr_read_b32 v18, a232\n"
"v_accvgpr_read_b32 v19, a233\n"
"v_max_u32 v8, v18, v19\n"
"v_accvgpr_read_b32 v18, a234\n"
"v_accvgpr_read_b32 v19, a235\n"
"v_max3_u32 v8, v18, v19, v8\n"
"buffer_load_dwordx4 a[148:151], v1, s[48:51], s45 offen offset:2048 sc1\n"
"v_mfma_f32_32x32x16_bf16 v[96:111], v[60:63], a[224:227], v[96:111]\n"
"ds_read_b128 v[56:59], v1 offset:27648\n"
"s_waitcnt vmcnt(13)\n"
"v_accvgpr_read_b32 v18, a236\n"
"v_accvgpr_read_b32 v19, a237\n"
"v_max3_u32 v8, v18, v19, v8\n"
"v_accvgpr_read_b32 v18, a238\n"
"v_accvgpr_read_b32 v19, a239\n"
"v_max3_u32 v8, v18, v19, v8\n"
"v_cmp_eq_u32 vcc, -1, v8\n"
"buffer_load_dwordx4 a[152:155], v1, s[48:51], s46 offen offset:2048 sc1\n"
"s_waitcnt lgkmcnt(3)\n"
"v_mfma_f32_32x32x16_bf16 v[80:95], v[64:67], a[228:231], v[80:95]\n"
"ds_read_b128 v[60:63], v1 offset:60416\n"
"v_mfma_f32_32x32x16_bf16 v[96:111], v[68:71], a[228:231], v[96:111]\n"
"ds_read_b128 v[64:67], v1 offset:28672\n"
"s_cbranch_vccnz .Lsl52\n"
".Lbk53:\n"
"buffer_load_dwordx4 a[156:159], v1, s[48:51], s47 offen offset:2048 sc1\n"
"s_waitcnt lgkmcnt(3)\n"
"v_mfma_f32_32x32x16_bf16 v[80:95], v[72:75], a[232:235], v[80:95]\n"
"ds_read_b128 v[68:71], v1 offset:61440\n"
"s_waitcnt vmcnt(14)\n"
"v_accvgpr_read_b32 v18, a240\n"
"v_accvgpr_read_b32 v19, a241\n"
"v_max_u32 v8, v18, v19\n"
"v_accvgpr_read_b32 v18, a242\n"
"v_accvgpr_read_b32 v19, a243\n"
"v_max3_u32 v8, v18, v19, v8\n"
"v_mfma_f32_32x32x16_bf16 v[96:111], v[76:79], a[232:235], v[96:111]\n"
"ds_read_b128 v[72:75], v1 offset:29696\n"
"s_waitcnt vmcnt(13)\n"
"v_accvgpr_read_b32 v18, a244\n"
"v_accvgpr_read_b32 v19, a245\n"
"v_max3_u32 v8, v18, v19, v8\n"
"v_accvgpr_read_b32 v18, a246\n"
"v_accvgpr_read_b32 v19, a247\n"
"v_max3_u32 v8, v18, v19, v8\n"
"v_cmp_eq_u32 vcc, -1, v8\n"
"buffer_load_dwordx4 a[160:163], v1, s[48:51], s40 offen offset:3072 sc1\n"
"s_waitcnt lgkmcnt(3)\n"
"v_mfma_f32_32x32x16_bf16 v[80:95], v[56:59], a[236:239], v[80:95]\n"
"ds_read_b128 v[76:79], v1 offset:62464\n"
"v_mfma_f32_32x32x16_bf16 v[96:111], v[60:63], a[236:239], v[96:111]\n"
"ds_read_b128 v[56:59], v1 offset:30720\n"
"s_cbranch_vccnz .Lsl54\n"
".Lbk55:\n"
"buffer_load_dwordx4 a[164:167], v1, s[48:51], s41 offen offset:3072 sc1\n"
"s_waitcnt lgkmcnt(3)\n"
"v_mfma_f32_32x32x16_bf16 v[80:95], v[64:67], a[240:243], v[80:95]\n"
"ds_read_b128 v[60:63], v1 offset:63488\n"
"s_waitcnt vmcnt(14)\n"
"v_accvgpr_read_b32 v18, a248\n"
"v_accvgpr_read_b32 v19, a249\n"
"v_max_u32 v8, v18, v19\n"
"v_accvgpr_read_b32 v18, a250\n"
"v_accvgpr_read_b32 v19, a251\n"
"v_max3_u32 v8, v18, v19, v8\n"
"v_mfma_f32_32x32x16_bf16 v[96:111], v[68:71], a[240:243], v[96:111]\n"
"ds_read_b128 v[64:67], v1 offset:31744\n"
"s_waitcnt vmcnt(13)\n"
"v_accvgpr_read_b32 v18, a252\n"
"v_accvgpr_read_b32 v19, a253\n"
"v_max3_u32 v8, v18, v19, v8\n"
"v_accvgpr_read_b32 v18, a254\n"
"v_accvgpr_read_b32 v19, a255\n"
"v_max3_u32 v8, v18, v19, v8\n"
"v_cmp_eq_u32 vcc, -1, v8\n"
"buffer_load_dwordx4 a[168:171], v1, s[48:51], s42 offen offset:3072 sc1\n"
"s_waitcnt lgkmcnt(3)\n"
"v_mfma_f32_32x32x16_bf16 v[80:95], v[72:75], a[244:247], v[80:95]\n"
"ds_read_b128 v[68:71], v1 offset:64512\n"
"buffer_load_dwordx4 a[172:175], v1, s[48:51], s43 offen offset:3072 sc1\n"
"s_barrier\n"
"v_mfma_f32_32x32x16_bf16 v[96:111], v[76:79], a[244:247], v[96:111]\n"
"ds_read_b128 v[72:75], v12\n"
"s_cbranch_vccnz .Lsl56\n"
".Lbk57:\n"
"s_waitcnt lgkmcnt(3)\n"
"v_mfma_f32_32x32x16_bf16 v[80:95], v[56:59], a[248:251], v[80:95]\n"
"ds_read_b128 v[76:79], v12 offset:1024\n"
"s_add_u32 m0, s61, 0\n"
"s_add_u32 s101, s60, 131072\n"
"buffer_load_dwordx4 v1, s[48:51], s101 offen sc1 lds\n"
"v_mfma_f32_32x32x16_bf16 v[96:111], v[60:63], a[248:251], v[96:111]\n"
"ds_read_b128 v[56:59], v12 offset:2048\n"
"buffer_store_dwordx4 v[240:243], v1, s[48:51], s39 offen offset:2048 sc1\n"
"s_waitcnt lgkmcnt(3)\n"
"v_mfma_f32_32x32x16_bf16 v[80:95], v[64:67], a[252:255], v[80:95]\n"
"ds_read_b128 v[60:63], v12 offset:3072\n"
"s_waitcnt vmcnt(16)\n"
"v_max3_u32 v9, v40, v41, v42\n"
"s_waitcnt vmcnt(15)\n"
"v_max3_u32 v9, v43, v44, v9\n"
"v_mfma_f32_32x32x16_bf16 v[96:111], v[68:71], a[252:255], v[96:111]\n"
"ds_read_b128 v[64:67], v12 offset:4096\n"
"v_max3_u32 v9, v45, v46, v9\n"
"s_waitcnt vmcnt(14)\n"
"v_max3_u32 v9, v47, v48, v9\n"
"v_max3_u32 v9, v49, v50, v9\n"
"s_waitcnt vmcnt(13)\n"
"v_max3_u32 v9, v51, v52, v9\n"
"v_max3_u32 v9, v53, v54, v9\n"
"s_waitcnt lgkmcnt(3)\n"
"v_mfma_f32_32x32x16_bf16 v[112:127], v[72:75], v[128:131], 0\n"
"ds_read_b128 v[68:71], v12 offset:5120\n"
"v_max_u32 v9, v55, v9\n"
"v_cmp_eq_u32 s[86:87], -1, v9\n"
"s_nop 1\n"
"s_cmp_lg_u64 s[86:87], 0\n"
"s_cbranch_scc1 .Lsl58\n"
".Lbk59:\n"
"v_mfma_f32_32x32x16_bf16 v[112:127], v[76:79], v[132:135], v[112:127]\n"
"ds_read_b128 v[72:75], v12 offset:6144\n"
"buffer_load_dwordx4 v1, s[48:51], s101 offen offset:1024 sc1 lds\n"
"v_add_f32 v40, v40, v48\n"
"v_add_f32 v41, v41, v49\n"
"v_add_f32 v42, v42, v50\n"
"v_add_f32 v43, v43, v51\n"
"s_waitcnt lgkmcnt(3)\n"
"v_mfma_f32_32x32x16_bf16 v[112:127], v[56:59], v[136:139], v[112:127]\n"
"ds_read_b128 v[76:79], v12 offset:7168\n"
"v_add_f32 v44, v44, v52\n"
"v_add_f32 v45, v45, v53\n"
"v_add_f32 v46, v46, v54\n"
"v_add_f32 v47, v47, v55\n"
"v_mfma_f32_32x32x16_bf16 v[112:127], v[60:63], v[140:143], v[112:127]\n"
"ds_read_b128 v[56:59], v12 offset:8192\n"
"buffer_load_dwordx4 v1, s[48:51], s101 offen offset:2048 sc1 lds\n"
"v_add_f32 v40, v40, v44\n"
"v_add_f32 v41, v41, v45\n"
"v_add_f32 v42, v42, v46\n"
"v_add_f32 v43, v43, v47\n"
"s_waitcnt lgkmcnt(3)\n"
"v_mfma_f32_32x32x16_bf16 v[112:127], v[64:67], v[144:147], v[112:127]\n"
"ds_read_b128 v[60:63], v12 offset:9216\n"
"v_permlane32_swap_b32 v40, v41\n"
"v_permlane32_swap_b32 v42, v43\n"
"v_add_f32 v40, v40, v41\n"
"v_add_f32 v42, v42, v43\n"
"v_mfma_f32_32x32x16_bf16 v[112:127], v[68:71], v[148:151], v[112:127]\n"
"ds_read_b128 v[64:67], v12 offset:10240\n"
"buffer_load_dwordx4 v1, s[48:51], s101 offen offset:3072 sc1 lds\n"
"v_permlane16_swap_b32 v40, v42\n"
"v_add_f32 v40, v40, v42\n"
"v_mov_b32 v41, v40\n"
"s_nop 1\n"
"v_permlane32_swap_b32 v40, v41\n"
"s_waitcnt lgkmcnt(3)\n"
"v_mfma_f32_32x32x16_bf16 v[112:127], v[72:75], v[152:155], v[112:127]\n"
"ds_read_b128 v[68:71], v12 offset:11264\n"
"v_mul_f32 v15, 0xbb000000, v40\n"
"v_mul_f32 v17, 0x3b000000, v41\n"
"v_fma_f32 v17, -v15, v15, v17\n"
"v_max_f32 v17, 0, v17\n"
"v_mfma_f32_32x32x16_bf16 v[112:127], v[76:79], v[156:159], v[112:127]\n"
"ds_read_b128 v[72:75], v12 offset:12288\n"
"s_add_u32 m0, s61, 32768\n"
"s_add_u32 s101, s59, 163840\n"
"buffer_load_dwordx4 v1, s[48:51], s101 offen sc1 lds\n"
"v_add_f32 v17, 0x3727c5ac, v17\n"
"buffer_store_dwordx4 v[240:243], v1, s[48:51], s39 offen offset:3072 sc1\n"
"s_waitcnt lgkmcnt(3)\n"
"v_mfma_f32_32x32x16_bf16 v[112:127], v[56:59], v[160:163], v[112:127]\n"
"ds_read_b128 v[76:79], v12 offset:13312\n"
"v_rsq_f32 v16, v17\n"
"s_nop 0\n"
"v_mul_f32 v16, 0xbfb8aa3b, v16\n"
"v_mfma_f32_32x32x16_bf16 v[112:127], v[60:63], v[164:167], v[112:127]\n"
"ds_read_b128 v[56:59], v12 offset:14336\n"
"ds_read_b128 v[40:43], v14\n"
"ds_read_b128 v[48:51], v14 offset:32\n"
"ds_read_b128 v[44:47], v14 offset:16\n"
"ds_read_b128 v[52:55], v14 offset:48\n"
"s_waitcnt lgkmcnt(3)\n"
"v_fmac_f32 v80, v15, v40\n"
"v_mfma_f32_32x32x16_bf16 v[112:127], v[64:67], v[168:171], v[112:127]\n"
"ds_read_b128 v[60:63], v12 offset:15360\n"
"v_mul_f32 v18, v16, v80\n"
"s_waitcnt lgkmcnt(3)\n"
"v_fmac_f32 v88, v15, v48\n"
"v_exp_f32 v18, v18\n"
"v_mfma_f32_32x32x16_bf16 v[112:127], v[68:71], v[172:175], v[112:127]\n"
"ds_read_b128 v[64:67], v12 offset:16384\n"
"buffer_load_dwordx4 v1, s[48:51], s101 offen offset:1024 sc1 lds\n"
"v_fmac_f32 v81, v15, v41\n"
"v_mul_f32 v19, v16, v81\n"
"v_fmac_f32 v89, v15, v49\n"
"v_mfma_f32_32x32x16_bf16 v[112:127], v[72:75], v[176:179], v[112:127]\n"
"ds_read_b128 v[68:71], v12 offset:17408\n"
"v_exp_f32 v19, v19\n"
"v_mul_f32 v80, v80, v88\n"
"v_fma_f32 v18, v18, v17, v17\n"
"v_mfma_f32_32x32x16_bf16 v[112:127], v[76:79], v[180:183], v[112:127]\n"
"ds_read_b128 v[72:75], v12 offset:18432\n"
"buffer_load_dwordx4 v1, s[48:51], s101 offen offset:2048 sc1 lds\n"
"v_rcp_f32 v18, v18\n"
"s_nop 0\n"
"v_mul_f32 v80, v80, v18\n"
"v_fmac_f32 v82, v15, v42\n"
"s_waitcnt lgkmcnt(3)\n"
"v_mfma_f32_32x32x16_bf16 v[112:127], v[56:59], v[184:187], v[112:127]\n"
"ds_read_b128 v[76:79], v12 offset:19456\n"
"v_mul_f32 v11, v16, v82\n"
"v_fmac_f32 v90, v15, v50\n"
"v_exp_f32 v11, v11\n"
"v_mfma_f32_32x32x16_bf16 v[112:127], v[60:63], v[188:191], v[112:127]\n"
"ds_read_b128 v[56:59], v12 offset:20480\n"
"buffer_load_dwordx4 v1, s[48:51], s101 offen offset:3072 sc1 lds\n"
"v_mul_f32 v81, v81, v89\n"
"v_fma_f32 v19, v19, v17, v17\n"
"v_rcp_f32 v19, v19\n"
"s_waitcnt lgkmcnt(3)\n"
"v_mfma_f32_32x32x16_bf16 v[112:127], v[64:67], v[192:195], v[112:127]\n"
"ds_read_b128 v[60:63], v12 offset:21504\n"
"v_mul_f32 v81, v81, v19\n"
"v_cvt_pk_bf16_f32 v32, v80, v81\n"
"v_fmac_f32 v83, v15, v43\n"
"v_mul_f32 v18, v16, v83\n"
"v_mfma_f32_32x32x16_bf16 v[112:127], v[68:71], v[196:199], v[112:127]\n"
"ds_read_b128 v[64:67], v12 offset:22528\n"
"v_fmac_f32 v91, v15, v51\n"
"v_exp_f32 v18, v18\n"
"v_mul_f32 v82, v82, v90\n"
"v_fma_f32 v11, v11, v17, v17\n"
"buffer_store_dwordx4 v[240:243], v30, s[48:51], s39 offen sc1\n"
"s_waitcnt lgkmcnt(3)\n"
"v_mfma_f32_32x32x16_bf16 v[112:127], v[72:75], v[200:203], v[112:127]\n"
"ds_read_b128 v[68:71], v12 offset:23552\n"
"v_rcp_f32 v11, v11\n"
"s_nop 0\n"
"v_mul_f32 v82, v82, v11\n"
"v_mfma_f32_32x32x16_bf16 v[112:127], v[76:79], v[204:207], v[112:127]\n"
"ds_read_b128 v[72:75], v12 offset:24576\n"
"v_fmac_f32 v84, v15, v44\n"
"v_mul_f32 v19, v16, v84\n"
"v_fmac_f32 v92, v15, v52\n"
"v_exp_f32 v19, v19\n"
"s_waitcnt lgkmcnt(3)\n"
"v_mfma_f32_32x32x16_bf16 v[112:127], v[56:59], v[208:211], v[112:127]\n"
"ds_read_b128 v[76:79], v12 offset:25600\n"
"buffer_load_dwordx4 a[176:179], v1, s[48:51], s44 offen offset:3072 sc1\n"
"v_mul_f32 v83, v83, v91\n"
"v_fma_f32 v18, v18, v17, v17\n"
"v_mfma_f32_32x32x16_bf16 v[112:127], v[60:63], v[212:215], v[112:127]\n"
"ds_read_b128 v[56:59], v12 offset:26624\n"
"v_rcp_f32 v18, v18\n"
"s_nop 0\n"
"v_mul_f32 v83, v83, v18\n"
"v_cvt_pk_bf16_f32 v33, v82, v83\n"
"v_fmac_f32 v85, v15, v45\n"
"s_waitcnt lgkmcnt(3)\n"
"v_mfma_f32_32x32x16_bf16 v[112:127], v[64:67], v[216:219], v[112:127]\n"
"ds_read_b128 v[60:63], v12 offset:27648\n"
"buffer_load_dwordx4 a[180:183], v1, s[48:51], s45 offen offset:3072 sc1\n"
"v_mul_f32 v11, v16, v85\n"
"v_fmac_f32 v93, v15, v53\n"
"v_mfma_f32_32x32x16_bf16 v[112:127], v[68:71], v[220:223], v[112:127]\n"
"ds_read_b128 v[64:67], v12 offset:28672\n"
"v_exp_f32 v11, v11\n"
"v_mul_f32 v84, v84, v92\n"
"v_fma_f32 v19, v19, v17, v17\n"
"s_waitcnt lgkmcnt(3)\n"
"v_mfma_f32_32x32x16_bf16 v[112:127], v[72:75], a[224:227], v[112:127]\n"
"ds_read_b128 v[68:71], v12 offset:29696\n"
"buffer_load_dwordx4 a[184:187], v1, s[48:51], s46 offen offset:3072 sc1\n"
"v_rcp_f32 v19, v19\n"
"s_nop 0\n"
"v_mul_f32 v84, v84, v19\n"
"v_mfma_f32_32x32x16_bf16 v[112:127], v[76:79], a[228:231], v[112:127]\n"
"ds_read_b128 v[72:75], v12 offset:30720\n"
"v_fmac_f32 v86, v15, v46\n"
"v_mul_f32 v18, v16, v86\n"
"v_fmac_f32 v94, v15, v54\n"
"v_exp_f32 v18, v18\n"
"s_waitcnt lgkmcnt(3)\n"
"v_mfma_f32_32x32x16_bf16 v[112:127], v[56:59], a[232:235], v[112:127]\n"
"ds_read_b128 v[76:79], v12 offset:31744\n"
"buffer_load_dwordx4 a[188:191], v1, s[48:51], s47 offen offset:3072 sc1\n"
"v_mul_f32 v85, v85, v93\n"
"v_fma_f32 v11, v11, v17, v17\n"
"s_waitcnt vmcnt(22)\n"
"s_barrier\n"
"v_mfma_f32_32x32x16_bf16 v[112:127], v[60:63], a[236:239], v[112:127]\n"
"ds_read_b128 v[56:59], v12 offset:32768\n"
"v_rcp_f32 v11, v11\n"
"s_nop 0\n"
"v_mul_f32 v85, v85, v11\n"
"buffer_store_dwordx4 v[240:243], v30, s[48:51], s39 offen offset:1024 sc1\n"
"s_waitcnt lgkmcnt(3)\n"
"v_mfma_f32_32x32x16_bf16 v[112:127], v[64:67], a[240:243], v[112:127]\n"
"ds_read_b128 v[60:63], v12 offset:33792\n"
"v_cvt_pk_bf16_f32 v34, v84, v85\n"
"v_fmac_f32 v87, v15, v47\n"
"v_mul_f32 v19, v16, v87\n"
"v_mfma_f32_32x32x16_bf16 v[112:127], v[68:71], a[244:247], v[112:127]\n"
"ds_read_b128 v[64:67], v12 offset:34816\n"
"v_fmac_f32 v95, v15, v55\n"
"v_exp_f32 v19, v19\n"
"v_mul_f32 v86, v86, v94\n"
"v_fma_f32 v18, v18, v17, v17\n"
"s_waitcnt lgkmcnt(3)\n"
"v_mfma_f32_32x32x16_bf16 v[112:127], v[72:75], a[248:251], v[112:127]\n"
"ds_read_b128 v[68:71], v12 offset:35840\n"
"s_add_u32 m0, s61, 16384\n"
"s_add_u32 s101, s60, 163840\n"
"buffer_load_dwordx4 v1, s[48:51], s101 offen sc1 lds\n"
"v_mfma_f32_32x32x16_bf16 v[112:127], v[76:79], a[252:255], v[112:127]\n"
"ds_read_b128 v[72:75], v12 offset:36864\n"
"v_rcp_f32 v18, v18\n"
"s_nop 0\n"
"v_mul_f32 v86, v86, v18\n"
"v_mul_f32 v87, v87, v95\n"
"v_fma_f32 v19, v19, v17, v17\n"
"s_waitcnt lgkmcnt(3)\n"
"v_mfma_f32_32x32x16_bf16 v[224:239], v[56:59], v[128:131], 0\n"
"ds_read_b128 v[76:79], v12 offset:37888\n"
"buffer_load_dwordx4 v1, s[48:51], s101 offen offset:1024 sc1 lds\n"
"v_rcp_f32 v19, v19\n"
"s_nop 0\n"
"v_mul_f32 v87, v87, v19\n"
"v_mfma_f32_32x32x16_bf16 v[224:239], v[60:63], v[132:135], v[224:239]\n"
"ds_read_b128 v[56:59], v12 offset:38912\n"
"v_cvt_pk_bf16_f32 v35, v86, v87\n"
"buffer_store_dwordx4 v[32:35], v1, s[48:51], s39 offen sc1\n"
"ds_read_b128 v[40:43], v14 offset:128\n"
"ds_read_b128 v[48:51], v14 offset:160\n"
"ds_read_b128 v[44:47], v14 offset:144\n"
"s_waitcnt lgkmcnt(6)\n"
"v_mfma_f32_32x32x16_bf16 v[224:239], v[64:67], v[136:139], v[224:239]\n"
"ds_read_b128 v[60:63], v12 offset:39936\n"
"ds_read_b128 v[52:55], v14 offset:176\n"
"s_waitcnt lgkmcnt(4)\n"
"v_fmac_f32 v96, v15, v40\n"
"v_mul_f32 v18, v16, v96\n"
"s_waitcnt lgkmcnt(3)\n"
"v_fmac_f32 v104, v15, v48\n"
"v_mfma_f32_32x32x16_bf16 v[224:239], v[68:71], v[140:143], v[224:239]\n"
"ds_read_b128 v[64:67], v12 offset:40960\n"
"v_exp_f32 v18, v18\n"
"v_fmac_f32 v97, v15, v41\n"
"v_mul_f32 v19, v16, v97\n"
"v_fmac_f32 v105, v15, v49\n"
"v_mfma_f32_32x32x16_bf16 v[224:239], v[72:75], v[144:147], v[224:239]\n"
"ds_read_b128 v[68:71], v12 offset:41984\n"
"v_exp_f32 v19, v19\n"
"v_mul_f32 v96, v96, v104\n"
"v_fma_f32 v18, v18, v17, v17\n"
"v_mfma_f32_32x32x16_bf16 v[224:239], v[76:79], v[148:151], v[224:239]\n"
"ds_read_b128 v[72:75], v12 offset:43008\n"
"buffer_load_dwordx4 v1, s[48:51], s101 offen offset:2048 sc1 lds\n"
"v_rcp_f32 v18, v18\n"
"s_nop 0\n"
"v_mul_f32 v96, v96, v18\n"
"v_fmac_f32 v98, v15, v42\n"
"buffer_store_dwordx4 v[240:243], v30, s[48:51], s39 offen offset:2048 sc1\n"
"s_waitcnt lgkmcnt(4)\n"
"v_mfma_f32_32x32x16_bf16 v[224:239], v[56:59], v[152:155], v[224:239]\n"
"ds_read_b128 v[76:79], v12 offset:44032\n"
"v_mul_f32 v11, v16, v98\n"
"v_fmac_f32 v106, v15, v50\n"
"v_mfma_f32_32x32x16_bf16 v[224:239], v[60:63], v[156:159], v[224:239]\n"
"ds_read_b128 v[56:59], v12 offset:45056\n"
"v_exp_f32 v11, v11\n"
"v_mul_f32 v97, v97, v105\n"
"v_fma_f32 v19, v19, v17, v17\n"
"s_waitcnt lgkmcnt(3)\n"
"v_mfma_f32_32x32x16_bf16 v[224:239], v[64:67], v[160:163], v[224:239]\n"
"ds_read_b128 v[60:63], v12 offset:46080\n"
"v_rcp_f32 v19, v19\n"
"s_nop 0\n"
"v_mul_f32 v97, v97, v19\n"
"v_cvt_pk_bf16_f32 v36, v96, v97\n"
"v_mfma_f32_32x32x16_bf16 v[224:239], v[68:71], v[164:167], v[224:239]\n"
"ds_read_b128 v[64:67], v12 offset:47104\n"
"buffer_load_dwordx4 v1, s[48:51], s101 offen offset:3072 sc1 lds\n"
"v_fmac_f32 v99, v15, v43\n"
"v_mul_f32 v18, v16, v99\n"
"v_fmac_f32 v107, v15, v51\n"
"s_waitcnt lgkmcnt(3)\n"
"v_mfma_f32_32x32x16_bf16 v[224:239], v[72:75], v[168:171], v[224:239]\n"
"ds_read_b128 v[68:71], v12 offset:48128\n"
"v_exp_f32 v18, v18\n"
"v_mul_f32 v98, v98, v106\n"
"v_fma_f32 v11, v11, v17, v17\n"
"v_mfma_f32_32x32x16_bf16 v[224:239], v[76:79], v[172:175], v[224:239]\n"
"ds_read_b128 v[72:75], v12 offset:49152\n"
"s_add_u32 m0, s61, 49152\n"
"s_add_u32 s101, s59, 196608\n"
"buffer_load_dwordx4 v1, s[48:51], s101 offen sc1 lds\n"
"v_rcp_f32 v11, v11\n"
"s_waitcnt lgkmcnt(3)\n"
"v_mfma_f32_32x32x16_bf16 v[224:239], v[56:59], v[176:179], v[224:239]\n"
"ds_read_b128 v[76:79], v12 offset:50176\n"
"v_mul_f32 v98, v98, v11\n"
"v_fmac_f32 v100, v15, v44\n"
"v_mul_f32 v19, v16, v100\n"
"v_fmac_f32 v108, v15, v52\n"
"v_mfma_f32_32x32x16_bf16 v[224:239], v[60:63], v[180:183], v[224:239]\n"
"ds_read_b128 v[56:59], v12 offset:51200\n"
"buffer_load_dwordx4 v1, s[48:51], s101 offen offset:1024 sc1 lds\n"
"v_exp_f32 v19, v19\n"
"v_mul_f32 v99, v99, v107\n"
"v_fma_f32 v18, v18, v17, v17\n"
"s_waitcnt lgkmcnt(3)\n"
"v_mfma_f32_32x32x16_bf16 v[224:239], v[64:67], v[184:187], v[224:239]\n"
"ds_read_b128 v[60:63], v12 offset:52224\n"
"v_rcp_f32 v18, v18\n"
"s_nop 0\n"
"v_mul_f32 v99, v99, v18\n"
"v_cvt_pk_bf16_f32 v37, v98, v99\n"
"v_mfma_f32_32x32x16_bf16 v[224:239], v[68:71], v[188:191], v[224:239]\n"
"ds_read_b128 v[64:67], v12 offset:53248\n"
"buffer_load_dwordx4 v1, s[48:51], s101 offen offset:2048 sc1 lds\n"
"v_fmac_f32 v101, v15, v45\n"
"v_mul_f32 v11, v16, v101\n"
"v_fmac_f32 v109, v15, v53\n"
"s_waitcnt lgkmcnt(3)\n"
"v_mfma_f32_32x32x16_bf16 v[224:239], v[72:75], v[192:195], v[224:239]\n"
"ds_read_b128 v[68:71], v12 offset:54272\n"
"v_exp_f32 v11, v11\n"
"v_mul_f32 v100, v100, v108\n"
"v_fma_f32 v19, v19, v17, v17\n"
"v_mfma_f32_32x32x16_bf16 v[224:239], v[76:79], v[196:199], v[224:239]\n"
"ds_read_b128 v[72:75], v12 offset:55296\n"
"buffer_load_dwordx4 v1, s[48:51], s101 offen offset:3072 sc1 lds\n"
"v_rcp_f32 v19, v19\n"
"s_nop 0\n"
"v_mul_f32 v100, v100, v19\n"
"v_fmac_f32 v102, v15, v46\n"
"s_waitcnt lgkmcnt(3)\n"
"v_mfma_f32_32x32x16_bf16 v[224:239], v[56:59], v[200:203], v[224:239]\n"
"ds_read_b128 v[76:79], v12 offset:56320\n"
"v_mul_f32 v18, v16, v102\n"
"v_fmac_f32 v110, v15, v54\n"
"v_exp_f32 v18, v18\n"
"v_mfma_f32_32x32x16_bf16 v[224:239], v[60:63], v[204:207], v[224:239]\n"
"ds_read_b128 v[56:59], v12 offset:57344\n"
"v_mul_f32 v101, v101, v109\n"
"v_fma_f32 v11, v11, v17, v17\n"
"v_rcp_f32 v11, v11\n"
"s_nop 0\n"
"v_mul_f32 v101, v101, v11\n"
"s_waitcnt lgkmcnt(3)\n"
"v_mfma_f32_32x32x16_bf16 v[224:239], v[64:67], v[208:211], v[224:239]\n"
"ds_read_b128 v[60:63], v12 offset:58368\n"
"s_add_u32 m0, s61, 65536\n"
"s_add_u32 s101, s60, 196608\n"
"buffer_load_dwordx4 v1, s[48:51], s101 offen sc1 lds\n"
"v_cvt_pk_bf16_f32 v38, v100, v101\n"
"v_mfma_f32_32x32x16_bf16 v[224:239], v[68:71], v[212:215], v[224:239]\n"
"ds_read_b128 v[64:67], v12 offset:59392\n"
"v_fmac_f32 v103, v15, v47\n"
"v_mul_f32 v19, v16, v103\n"
"v_fmac_f32 v111, v15, v55\n"
"v_exp_f32 v19, v19\n"
"s_waitcnt lgkmcnt(3)\n"
"v_mfma_f32_32x32x16_bf16 v[224:239], v[72:75], v[216:219], v[224:239]\n"
"ds_read_b128 v[68:71], v12 offset:60416\n"
"buffer_load_dwordx4 v1, s[48:51], s101 offen offset:1024 sc1 lds\n"
"v_mul_f32 v102, v102, v110\n"
"v_fma_f32 v18, v18, v17, v17\n"
"v_mfma_f32_32x32x16_bf16 v[224:239], v[76:79], v[220:223], v[224:239]\n"
"ds_read_b128 v[72:75], v12 offset:61440\n"
"v_rcp_f32 v18, v18\n"
"s_nop 0\n"
"v_mul_f32 v102, v102, v18\n"
"v_mul_f32 v103, v103, v111\n"
"v_fma_f32 v19, v19, v17, v17\n"
"s_waitcnt lgkmcnt(3)\n"
"v_mfma_f32_32x32x16_bf16 v[224:239], v[56:59], a[224:227], v[224:239]\n"
"ds_read_b128 v[76:79], v12 offset:62464\n"
"buffer_load_dwordx4 v1, s[48:51], s101 offen offset:2048 sc1 lds\n"
"v_rcp_f32 v19, v19\n"
"s_nop 0\n"
"v_mul_f32 v103, v103, v19\n"
"v_mfma_f32_32x32x16_bf16 v[224:239], v[60:63], a[228:231], v[224:239]\n"
"ds_read_b128 v[56:59], v12 offset:63488\n"
"v_cvt_pk_bf16_f32 v39, v102, v103\n"
"buffer_store_dwordx4 v[36:39], v1, s[48:51], s39 offen offset:1024 sc1\n"
"ds_read_b128 v[40:43], v14 offset:256\n"
"ds_read_b128 v[48:51], v14 offset:288\n"
"ds_read_b128 v[44:47], v14 offset:272\n"
"s_waitcnt lgkmcnt(6)\n"
"v_mfma_f32_32x32x16_bf16 v[224:239], v[64:67], a[232:235], v[224:239]\n"
"ds_read_b128 v[60:63], v12 offset:64512\n"
"ds_read_b128 v[52:55], v14 offset:304\n"
"s_waitcnt lgkmcnt(4)\n"
"v_fmac_f32 v112, v15, v40\n"
"v_mul_f32 v18, v16, v112\n"
"s_waitcnt lgkmcnt(3)\n"
"v_fmac_f32 v120, v15, v48\n"
"v_mfma_f32_32x32x16_bf16 v[224:239], v[68:71], a[236:239], v[224:239]\n"
"ds_read_b128 v[64:67], v13\n"
"v_exp_f32 v18, v18\n"
"v_fmac_f32 v113, v15, v41\n"
"v_mul_f32 v19, v16, v113\n"
"v_fmac_f32 v121, v15, v49\n"
"v_mfma_f32_32x32x16_bf16 v[224:239], v[72:75], a[240:243], v[224:239]\n"
"ds_read_b128 v[68:71], v13 offset:1024\n"
"buffer_load_dwordx4 v1, s[48:51], s101 offen offset:3072 sc1 lds\n"
"v_exp_f32 v19, v19\n"
"v_mul_f32 v112, v112, v120\n"
"v_mfma_f32_32x32x16_bf16 v[224:239], v[76:79], a[244:247], v[224:239]\n"
"ds_read_b128 v[72:75], v13 offset:2048\n"
"v_fma_f32 v18, v18, v17, v17\n"
"v_rcp_f32 v18, v18\n"
"s_nop 0\n"
"v_mul_f32 v112, v112, v18\n"
"v_fmac_f32 v114, v15, v42\n"
"s_waitcnt lgkmcnt(4)\n"
"v_mfma_f32_32x32x16_bf16 v[224:239], v[56:59], a[248:251], v[224:239]\n"
"ds_read_b128 v[76:79], v13 offset:3072\n"
"buffer_load_dwordx4 a[192:195], v30, s[48:51], s40 offen sc1\n"
"v_mul_f32 v11, v16, v114\n"
"v_fmac_f32 v122, v15, v50\n"
"v_mfma_f32_32x32x16_bf16 v[224:239], v[60:63], a[252:255], v[224:239]\n"
"ds_read_b128 v[56:59], v13 offset:4096\n"
"v_exp_f32 v11, v11\n"
"v_mul_f32 v113, v113, v121\n"
"v_fma_f32 v19, v19, v17, v17\n"
"s_waitcnt lgkmcnt(3)\n"
"v_mfma_f32_32x32x16_bf16 v[80:95], v[64:67], v[128:131], 0\n"
"ds_read_b128 v[60:63], v13 offset:5120\n"
"buffer_load_dwordx4 a[196:199], v30, s[48:51], s41 offen sc1\n"
"v_rcp_f32 v19, v19\n"
"s_nop 0\n"
"v_mul_f32 v113, v113, v19\n"
"v_mfma_f32_32x32x16_bf16 v[80:95], v[68:71], v[132:135], v[80:95]\n"
"ds_read_b128 v[64:67], v13 offset:6144\n"
"v_cvt_pk_bf16_f32 v32, v112, v113\n"
"v_fmac_f32 v115, v15, v43\n"
"v_mul_f32 v18, v16, v115\n"
"v_fmac_f32 v123, v15, v51\n"
"s_waitcnt lgkmcnt(3)\n"
"v_mfma_f32_32x32x16_bf16 v[80:95], v[72:75], v[136:139], v[80:95]\n"
"ds_read_b128 v[68:71], v13 offset:7168\n"
"buffer_load_dwordx4 a[200:203], v30, s[48:51], s42 offen sc1\n"
"v_exp_f32 v18, v18\n"
"v_mul_f32 v114, v114, v122\n"
"v_mfma_f32_32x32x16_bf16 v[80:95], v[76:79], v[140:143], v[80:95]\n"
"ds_read_b128 v[72:75], v13 offset:8192\n"
"v_fma_f32 v11, v11, v17, v17\n"
"v_rcp_f32 v11, v11\n"
"s_nop 0\n"
"v_mul_f32 v114, v114, v11\n"
"v_fmac_f32 v116, v15, v44\n"
"s_waitcnt lgkmcnt(3)\n"
"v_mfma_f32_32x32x16_bf16 v[80:95], v[56:59], v[144:147], v[80:95]\n"
"ds_read_b128 v[76:79], v13 offset:9216\n"
"v_mul_f32 v19, v16, v116\n"
"v_fmac_f32 v124, v15, v52\n"
"v_exp_f32 v19, v19\n"
"v_mfma_f32_32x32x16_bf16 v[80:95], v[60:63], v[148:151], v[80:95]\n"
"ds_read_b128 v[56:59], v13 offset:10240\n"
"buffer_load_dwordx4 a[204:207], v30, s[48:51], s43 offen sc1\n"
"v_mul_f32 v115, v115, v123\n"
"v_fma_f32 v18, v18, v17, v17\n"
"v_rcp_f32 v18, v18\n"
"s_waitcnt lgkmcnt(3)\n"
"v_mfma_f32_32x32x16_bf16 v[80:95], v[64:67], v[152:155], v[80:95]\n"
"ds_read_b128 v[60:63], v13 offset:11264\n"
"v_mul_f32 v115, v115, v18\n"
"v_cvt_pk_bf16_f32 v33, v114, v115\n"
"v_fmac_f32 v117, v15, v45\n"
"v_mul_f32 v11, v16, v117\n"
"v_mfma_f32_32x32x16_bf16 v[80:95], v[68:71], v[156:159], v[80:95]\n"
"ds_read_b128 v[64:67], v13 offset:12288\n"
"buffer_load_dwordx4 a[208:211], v30, s[48:51], s44 offen sc1\n"
"v_fmac_f32 v125, v15, v53\n"
"v_exp_f32 v11, v11\n"
"v_mul_f32 v116, v116, v124\n"
"s_waitcnt lgkmcnt(3)\n"
"v_mfma_f32_32x32x16_bf16 v[80:95], v[72:75], v[160:163], v[80:95]\n"
"ds_read_b128 v[68:71], v13 offset:13312\n"
"v_fma_f32 v19, v19, v17, v17\n"
"v_rcp_f32 v19, v19\n"
"s_nop 0\n"
"v_mul_f32 v116, v116, v19\n"
"v_mfma_f32_32x32x16_bf16 v[80:95], v[76:79], v[164:167], v[80:95]\n"
"ds_read_b128 v[72:75], v13 offset:14336\n"
"buffer_load_dwordx4 a[212:215], v30, s[48:51], s45 offen sc1\n"
"v_fmac_f32 v118, v15, v46\n"
"v_mul_f32 v18, v16, v118\n"
"v_fmac_f32 v126, v15, v54\n"
"s_waitcnt lgkmcnt(3)\n"
"v_mfma_f32_32x32x16_bf16 v[80:95], v[56:59], v[168:171], v[80:95]\n"
"ds_read_b128 v[76:79], v13 offset:15360\n"
"v_exp_f32 v18, v18\n"
"v_mul_f32 v117, v117, v125\n"
"v_fma_f32 v11, v11, v17, v17\n"
"s_waitcnt vmcnt(27)\n"
"s_waitcnt vmcnt(7)\n"
"s_barrier\n"
"s_cmp_lg_u32 s68, 1\n"
"s_cbranch_scc1 .Lpub60\n"
"s_mov_b64 exec, 1\n"
"buffer_store_dwordx2 v[28:29], off, s[48:51], s18 sc1\n"
"s_mov_b64 exec, -1\n"
".Lpub60:\n"
"v_mfma_f32_32x32x16_bf16 v[80:95], v[60:63], v[172:175], v[80:95]\n"
"ds_read_b128 v[56:59], v1\n"
"buffer_load_dwordx4 a[216:219], v30, s[48:51], s46 offen sc1\n"
"s_waitcnt lgkmcnt(3)\n"
"v_mfma_f32_32x32x16_bf16 v[80:95], v[64:67], v[176:179], v[80:95]\n"
"ds_read_b128 v[60:63], v1 offset:1024\n"
"v_rcp_f32 v11, v11\n"
"s_nop 0\n"
"v_mul_f32 v117, v117, v11\n"
"v_cvt_pk_bf16_f32 v34, v116, v117\n"
"v_mfma_f32_32x32x16_bf16 v[80:95], v[68:71], v[180:183], v[80:95]\n"
"ds_read_b128 v[64:67], v1 offset:2048\n"
"buffer_load_dwordx4 a[220:223], v30, s[48:51], s47 offen sc1\n"
"v_fmac_f32 v119, v15, v47\n"
"v_mul_f32 v19, v16, v119\n"
"v_fmac_f32 v127, v15, v55\n"
"s_waitcnt lgkmcnt(3)\n"
"v_mfma_f32_32x32x16_bf16 v[80:95], v[72:75], v[184:187], v[80:95]\n"
"ds_read_b128 v[68:71], v1 offset:3072\n"
"v_exp_f32 v19, v19\n"
"v_mul_f32 v118, v118, v126\n"
"v_fma_f32 v18, v18, v17, v17\n"
"v_mfma_f32_32x32x16_bf16 v[80:95], v[76:79], v[188:191], v[80:95]\n"
"ds_read_b128 v[72:75], v1 offset:4096\n"
"buffer_load_dwordx4 a[0:3], v30, s[48:51], s40 offen offset:1024 sc1\n"
"v_rcp_f32 v18, v18\n"
"s_nop 0\n"
"v_mul_f32 v118, v118, v18\n"
"v_mul_f32 v119, v119, v127\n"
"s_waitcnt lgkmcnt(3)\n"
"v_mfma_f32_32x32x16_bf16 v[80:95], v[56:59], v[192:195], v[80:95]\n"
"ds_read_b128 v[76:79], v1 offset:5120\n"
"v_fma_f32 v19, v19, v17, v17\n"
"v_rcp_f32 v19, v19\n"
"s_nop 0\n"
"v_mul_f32 v119, v119, v19\n"
"v_mfma_f32_32x32x16_bf16 v[80:95], v[60:63], v[196:199], v[80:95]\n"
"ds_read_b128 v[56:59], v1 offset:6144\n"
"buffer_load_dwordx4 a[4:7], v30, s[48:51], s41 offen offset:1024 sc1\n"
"v_cvt_pk_bf16_f32 v35, v118, v119\n"
"buffer_store_dwordx4 v[32:35], v1, s[48:51], s39 offen offset:2048 sc1\n"
"ds_read_b128 v[40:43], v14 offset:384\n"
"ds_read_b128 v[48:51], v14 offset:416\n"
"s_waitcnt lgkmcnt(5)\n"
"v_mfma_f32_32x32x16_bf16 v[80:95], v[64:67], v[200:203], v[80:95]\n"
"ds_read_b128 v[60:63], v1 offset:7168\n"
"ds_read_b128 v[44:47], v14 offset:400\n"
"ds_read_b128 v[52:55], v14 offset:432\n"
"s_waitcnt lgkmcnt(4)\n"
"v_fmac_f32 v224, v15, v40\n"
"v_mul_f32 v18, v16, v224\n"
"v_mfma_f32_32x32x16_bf16 v[80:95], v[68:71], v[204:207], v[80:95]\n"
"ds_read_b128 v[64:67], v1 offset:8192\n"
"s_waitcnt lgkmcnt(4)\n"
"v_fmac_f32 v232, v15, v48\n"
"v_exp_f32 v18, v18\n"
"v_fmac_f32 v225, v15, v41\n"
"v_mul_f32 v19, v16, v225\n"
"v_mfma_f32_32x32x16_bf16 v[80:95], v[72:75], v[208:211], v[80:95]\n"
"ds_read_b128 v[68:71], v1 offset:9216\n"
"v_fmac_f32 v233, v15, v49\n"
"v_exp_f32 v19, v19\n"
"v_mul_f32 v224, v224, v232\n"
"v_mfma_f32_32x32x16_bf16 v[80:95], v[76:79], v[212:215], v[80:95]\n"
"ds_read_b128 v[72:75], v1 offset:10240\n"
"buffer_load_dwordx4 a[8:11], v30, s[48:51], s42 offen offset:1024 sc1\n"
"v_fma_f32 v18, v18, v17, v17\n"
"v_rcp_f32 v18, v18\n"
"s_nop 0\n"
"v_mul_f32 v224, v224, v18\n"
"s_waitcnt lgkmcnt(5)\n"
"v_mfma_f32_32x32x16_bf16 v[80:95], v[56:59], v[216:219], v[80:95]\n"
"ds_read_b128 v[76:79], v1 offset:11264\n"
"v_fmac_f32 v226, v15, v42\n"
"v_mul_f32 v11, v16, v226\n"
"v_fmac_f32 v234, v15, v50\n"
"v_mfma_f32_32x32x16_bf16 v[80:95], v[60:63], v[220:223], v[80:95]\n"
"ds_read_b128 v[56:59], v1 offset:12288\n"
"v_exp_f32 v11, v11\n"
"v_mul_f32 v225, v225, v233\n"
"v_fma_f32 v19, v19, v17, v17\n"
"s_waitcnt lgkmcnt(3)\n"
"v_mfma_f32_32x32x16_bf16 v[80:95], v[64:67], a[224:227], v[80:95]\n"
"ds_read_b128 v[60:63], v1 offset:13312\n"
"buffer_load_dwordx4 a[12:15], v30, s[48:51], s43 offen offset:1024 sc1\n"
"v_rcp_f32 v19, v19\n"
"s_nop 0\n"
"v_mul_f32 v225, v225, v19\n"
"v_mfma_f32_32x32x16_bf16 v[80:95], v[68:71], a[228:231], v[80:95]\n"
"ds_read_b128 v[64:67], v1 offset:14336\n"
"v_cvt_pk_bf16_f32 v36, v224, v225\n"
"v_fmac_f32 v227, v15, v43\n"
"v_mul_f32 v18, v16, v227\n"
"v_fmac_f32 v235, v15, v51\n"
"s_waitcnt lgkmcnt(3)\n"
"v_mfma_f32_32x32x16_bf16 v[80:95], v[72:75], a[232:235], v[80:95]\n"
"ds_read_b128 v[68:71], v1 offset:15360\n"
"buffer_load_dwordx4 a[16:19], v30, s[48:51], s44 offen offset:1024 sc1\n"
"v_exp_f32 v18, v18\n"
"v_mul_f32 v226, v226, v234\n"
"v_mfma_f32_32x32x16_bf16 v[80:95], v[76:79], a[236:239], v[80:95]\n"
"ds_read_b128 v[72:75], v1 offset:32768\n"
"v_fma_f32 v11, v11, v17, v17\n"
"v_rcp_f32 v11, v11\n"
"s_nop 0\n"
"v_mul_f32 v226, v226, v11\n"
"v_fmac_f32 v228, v15, v44\n"
"s_waitcnt lgkmcnt(3)\n"
"v_mfma_f32_32x32x16_bf16 v[80:95], v[56:59], a[240:243], v[80:95]\n"
"ds_read_b128 v[76:79], v1 offset:33792\n"
"buffer_load_dwordx4 a[20:23], v30, s[48:51], s45 offen offset:1024 sc1\n"
"v_mul_f32 v19, v16, v228\n"
"v_fmac_f32 v236, v15, v52\n"
"v_mfma_f32_32x32x16_bf16 v[80:95], v[60:63], a[244:247], v[80:95]\n"
"ds_read_b128 v[56:59], v1 offset:34816\n"
"v_exp_f32 v19, v19\n"
"v_mul_f32 v227, v227, v235\n"
"v_fma_f32 v18, v18, v17, v17\n"
"s_waitcnt lgkmcnt(3)\n"
"v_mfma_f32_32x32x16_bf16 v[80:95], v[64:67], a[248:251], v[80:95]\n"
"ds_read_b128 v[60:63], v1 offset:35840\n"
"buffer_load_dwordx4 a[24:27], v30, s[48:51], s46 offen offset:1024 sc1\n"
"v_rcp_f32 v18, v18\n"
"s_nop 0\n"
"v_mul_f32 v227, v227, v18\n"
"v_mfma_f32_32x32x16_bf16 v[80:95], v[68:71], a[252:255], v[80:95]\n"
"ds_read_b128 v[64:67], v1 offset:36864\n"
"v_cvt_pk_bf16_f32 v37, v226, v227\n"
"v_fmac_f32 v229, v15, v45\n"
"v_mul_f32 v11, v16, v229\n"
"v_fmac_f32 v237, v15, v53\n"
"s_waitcnt lgkmcnt(3)\n"
"v_mfma_f32_32x32x16_bf16 v[96:111], v[72:75], v[128:131], 0\n"
"ds_read_b128 v[68:71], v1 offset:37888\n"
"buffer_load_dwordx4 a[28:31], v30, s[48:51], s47 offen offset:1024 sc1\n"
"v_exp_f32 v11, v11\n"
"v_mul_f32 v228, v228, v236\n"
"v_mfma_f32_32x32x16_bf16 v[96:111], v[76:79], v[132:135], v[96:111]\n"
"ds_read_b128 v[72:75], v1 offset:38912\n"
"v_fma_f32 v19, v19, v17, v17\n"
"v_rcp_f32 v19, v19\n"
"s_nop 0\n"
"v_mul_f32 v228, v228, v19\n"
"v_fmac_f32 v230, v15, v46\n"
"s_waitcnt lgkmcnt(3)\n"
"v_mfma_f32_32x32x16_bf16 v[96:111], v[56:59], v[136:139], v[96:111]\n"
"ds_read_b128 v[76:79], v1 offset:39936\n"
"buffer_load_dwordx4 a[32:35], v30, s[48:51], s40 offen offset:2048 sc1\n"
"v_mul_f32 v18, v16, v230\n"
"v_fmac_f32 v238, v15, v54\n"
"v_mfma_f32_32x32x16_bf16 v[96:111], v[60:63], v[140:143], v[96:111]\n"
"ds_read_b128 v[56:59], v1 offset:40960\n"
"v_exp_f32 v18, v18\n"
"v_mul_f32 v229, v229, v237\n"
"v_fma_f32 v11, v11, v17, v17\n"
"s_waitcnt lgkmcnt(3)\n"
"v_mfma_f32_32x32x16_bf16 v[96:111], v[64:67], v[144:147], v[96:111]\n"
"ds_read_b128 v[60:63], v1 offset:41984\n"
"buffer_load_dwordx4 a[36:39], v30, s[48:51], s41 offen offset:2048 sc1\n"
"v_rcp_f32 v11, v11\n"
"s_nop 0\n"
"v_mul_f32 v229, v229, v11\n"
"s_cmp_eq_u32 s68, 2\n"
"s_cselect_b64 exec, -1, 0\n"
"buffer_load_dwordx2 v[244:245], v6, s[48:51], s85 offen sc1\n"
"s_mov_b64 exec, -1\n"
"v_mfma_f32_32x32x16_bf16 v[96:111], v[68:71], v[148:151], v[96:111]\n"
"ds_read_b128 v[64:67], v1 offset:43008\n"
"v_cvt_pk_bf16_f32 v38, v228, v229\n"
"s_waitcnt lgkmcnt(3)\n"
"v_mfma_f32_32x32x16_bf16 v[96:111], v[72:75], v[152:155], v[96:111]\n"
"ds_read_b128 v[68:71], v1 offset:44032\n"
"buffer_load_dwordx4 a[40:43], v30, s[48:51], s42 offen offset:2048 sc1\n"
"v_fmac_f32 v231, v15, v47\n"
"v_mul_f32 v19, v16, v231\n"
"v_fmac_f32 v239, v15, v55\n"
"v_mfma_f32_32x32x16_bf16 v[96:111], v[76:79], v[156:159], v[96:111]\n"
"ds_read_b128 v[72:75], v1 offset:45056\n"
"v_exp_f32 v19, v19\n"
"v_mul_f32 v230, v230, v238\n"
"v_fma_f32 v18, v18, v17, v17\n"
"s_waitcnt lgkmcnt(3)\n"
"v_mfma_f32_32x32x16_bf16 v[96:111], v[56:59], v[160:163], v[96:111]\n"
"ds_read_b128 v[76:79], v1 offset:46080\n"
"v_rcp_f32 v18, v18\n"
"s_nop 0\n"
"v_mul_f32 v230, v230, v18\n"
"v_mul_f32 v231, v231, v239\n"
"v_mfma_f32_32x32x16_bf16 v[96:111], v[60:63], v[164:167], v[96:111]\n"
"ds_read_b128 v[56:59], v1 offset:47104\n"
"buffer_load_dwordx4 a[44:47], v30, s[48:51], s43 offen offset:2048 sc1\n"
"v_fma_f32 v19, v19, v17, v17\n"
"v_rcp_f32 v19, v19\n"
"s_nop 0\n"
"v_mul_f32 v231, v231, v19\n"
"s_waitcnt lgkmcnt(3)\n"
"v_mfma_f32_32x32x16_bf16 v[96:111], v[64:67], v[168:171], v[96:111]\n"
"ds_read_b128 v[60:63], v1 offset:48128\n"
"v_cvt_pk_bf16_f32 v39, v230, v231\n"
"buffer_store_dwordx4 v[36:39], v1, s[48:51], s39 offen offset:3072 sc1\n"
"ds_read_b128 v[40:43], v14 offset:512\n"
"ds_read_b128 v[48:51], v14 offset:544\n"
"s_waitcnt vmcnt(23)\n"
"s_barrier\n"
"v_mfma_f32_32x32x16_bf16 v[96:111], v[68:71], v[172:175], v[96:111]\n"
"ds_read_b128 v[64:67], v1 offset:16384\n"
"ds_read_b128 v[44:47], v14 offset:528\n"
"ds_read_b128 v[52:55], v14 offset:560\n"
"s_waitcnt lgkmcnt(4)\n"
"v_fmac_f32 v80, v15, v40\n"
"v_mfma_f32_32x32x16_bf16 v[96:111], v[72:75], v[176:179], v[96:111]\n"
"ds_read_b128 v[68:71], v1 offset:17408\n"
"v_mul_f32 v18, v16, v80\n"
"s_waitcnt lgkmcnt(4)\n"
"v_fmac_f32 v88, v15, v48\n"
"v_exp_f32 v18, v18\n"
"v_mfma_f32_32x32x16_bf16 v[96:111], v[76:79], v[180:183], v[96:111]\n"
"ds_read_b128 v[72:75], v1 offset:18432\n"
"buffer_load_dwordx4 a[48:51], v30, s[48:51], s44 offen offset:2048 sc1\n"
"v_fmac_f32 v81, v15, v41\n"
"v_mul_f32 v19, v16, v81\n"
"v_fmac_f32 v89, v15, v49\n"
"v_mfma_f32_32x32x16_bf16 v[96:111], v[56:59], v[184:187], v[96:111]\n"
"ds_read_b128 v[76:79], v1 offset:19456\n"
"v_exp_f32 v19, v19\n"
"v_mul_f32 v80, v80, v88\n"
"v_fma_f32 v18, v18, v17, v17\n"
"v_mfma_f32_32x32x16_bf16 v[96:111], v[60:63], v[188:191], v[96:111]\n"
"ds_read_b128 v[56:59], v1 offset:20480\n"
"buffer_load_dwordx4 a[52:55], v30, s[48:51], s45 offen offset:2048 sc1\n"
"v_rcp_f32 v18, v18\n"
"s_nop 0\n"
"v_mul_f32 v80, v80, v18\n"
"v_fmac_f32 v82, v15, v42\n"
"s_waitcnt lgkmcnt(3)\n"
"v_mfma_f32_32x32x16_bf16 v[96:111], v[64:67], v[192:195], v[96:111]\n"
"ds_read_b128 v[60:63], v1 offset:21504\n"
"v_mul_f32 v11, v16, v82\n"
"v_fmac_f32 v90, v15, v50\n"
"v_exp_f32 v11, v11\n"
"v_mfma_f32_32x32x16_bf16 v[96:111], v[68:71], v[196:199], v[96:111]\n"
"ds_read_b128 v[64:67], v1 offset:22528\n"
"buffer_load_dwordx4 a[56:59], v30, s[48:51], s46 offen offset:2048 sc1\n"
"v_mul_f32 v81, v81, v89\n"
"v_fma_f32 v19, v19, v17, v17\n"
"v_rcp_f32 v19, v19\n"
"s_waitcnt lgkmcnt(3)\n"
"v_mfma_f32_32x32x16_bf16 v[96:111], v[72:75], v[200:203], v[96:111]\n"
"ds_read_b128 v[68:71], v1 offset:23552\n"
"v_mul_f32 v81, v81, v19\n"
"v_cvt_pk_bf16_f32 v32, v80, v81\n"
"v_fmac_f32 v83, v15, v43\n"
"v_mul_f32 v18, v16, v83\n"
"v_mfma_f32_32x32x16_bf16 v[96:111], v[76:79], v[204:207], v[96:111]\n"
"ds_read_b128 v[72:75], v1 offset:24576\n"
"buffer_load_dwordx4 a[60:63], v30, s[48:51], s47 offen offset:2048 sc1\n"
"v_fmac_f32 v91, v15, v51\n"
"v_exp_f32 v18, v18\n"
"v_mul_f32 v82, v82, v90\n"
"s_waitcnt lgkmcnt(3)\n"
"v_mfma_f32_32x32x16_bf16 v[96:111], v[56:59], v[208:211], v[96:111]\n"
"ds_read_b128 v[76:79], v1 offset:25600\n"
"v_fma_f32 v11, v11, v17, v17\n"
"v_rcp_f32 v11, v11\n"
"s_nop 0\n"
"v_mul_f32 v82, v82, v11\n"
"v_mfma_f32_32x32x16_bf16 v[96:111], v[60:63], v[212:215], v[96:111]\n"
"ds_read_b128 v[56:59], v1 offset:26624\n"
"v_fmac_f32 v84, v15, v44\n"
"v_mul_f32 v19, v16, v84\n"
"v_fmac_f32 v92, v15, v52\n"
"v_exp_f32 v19, v19\n"
"s_waitcnt lgkmcnt(3)\n"
"v_mfma_f32_32x32x16_bf16 v[96:111], v[64:67], v[216:219], v[96:111]\n"
"ds_read_b128 v[60:63], v1 offset:27648\n"
"v_mul_f32 v83, v83, v91\n"
"v_fma_f32 v18, v18, v17, v17\n"
"v_rcp_f32 v18, v18\n"
"v_mfma_f32_32x32x16_bf16 v[96:111], v[68:71], v[220:223], v[96:111]\n"
"ds_read_b128 v[64:67], v1 offset:28672\n"
"v_mul_f32 v83, v83, v18\n"
"v_cvt_pk_bf16_f32 v33, v82, v83\n"
"v_fmac_f32 v85, v15, v45\n"
"v_mul_f32 v11, v16, v85\n"
"v_fmac_f32 v93, v15, v53\n"
"s_waitcnt lgkmcnt(3)\n"
"v_mfma_f32_32x32x16_bf16 v[96:111], v[72:75], a[224:227], v[96:111]\n"
"ds_read_b128 v[68:71], v1 offset:29696\n"
"v_exp_f32 v11, v11\n"
"v_mul_f32 v84, v84, v92\n"
"v_fma_f32 v19, v19, v17, v17\n"
"v_mfma_f32_32x32x16_bf16 v[96:111], v[76:79], a[228:231], v[96:111]\n"
"ds_read_b128 v[72:75], v1 offset:30720\n"
"v_rcp_f32 v19, v19\n"
"s_nop 0\n"
"v_mul_f32 v84, v84, v19\n"
"v_fmac_f32 v86, v15, v46\n"
"v_mul_f32 v18, v16, v86\n"
"s_cmp_eq_u32 s68, 2\n"
"s_cselect_b64 exec, -1, 0\n"
"s_waitcnt vmcnt(7)\n"
"v_cmp_ne_u64 vcc, v[244:245], s[72:73]\n"
"s_mov_b64 exec, -1\n"
"s_waitcnt lgkmcnt(3)\n"
"v_mfma_f32_32x32x16_bf16 v[96:111], v[56:59], a[232:235], v[96:111]\n"
"ds_read_b128 v[76:79], v1 offset:31744\n"
"v_mfma_f32_32x32x16_bf16 v[96:111], v[60:63], a[236:239], v[96:111]\n"
"ds_read_b128 v[56:59], v1 offset:49152\n"
"v_fmac_f32 v94, v15, v54\n"
"v_exp_f32 v18, v18\n"
"v_mul_f32 v85, v85, v93\n"
"v_fma_f32 v11, v11, v17, v17\n"
"s_cbranch_vccz .Lpd61\n"
"s_mov_b32 s83, 50000\n"
".Lpl62:\n"
"buffer_load_dwordx2 v[244:245], v6, s[48:51], s85 offen sc1\n"
"s_waitcnt vmcnt(0)\n"
"v_cmp_ne_u64 vcc, v[244:245], s[72:73]\n"
"s_sub_u32 s83, s83, 1\n"
"s_cbranch_scc1 .Lpd61\n"
"s_nop 2\n"
"s_cbranch_vccz .Lpd61\n"
"s_sleep 4\n"
"s_branch .Lpl62\n"
".Lpd61:\n"
"s_barrier\n"
"s_waitcnt lgkmcnt(3)\n"
"v_mfma_f32_32x32x16_bf16 v[96:111], v[64:67], a[240:243], v[96:111]\n"
"ds_read_b128 v[60:63], v1 offset:50176\n"
"buffer_load_dwordx4 v[224:227], v1, s[48:51], s20 offen sc1\n"
"v_mfma_f32_32x32x16_bf16 v[96:111], v[68:71], a[244:247], v[96:111]\n"
"ds_read_b128 v[64:67], v1 offset:51200\n"
"v_rcp_f32 v11, v11\n"
"s_nop 0\n"
"v_mul_f32 v85, v85, v11\n"
"v_cvt_pk_bf16_f32 v34, v84, v85\n"
"v_fmac_f32 v87, v15, v47\n"
"s_waitcnt lgkmcnt(3)\n"
"v_mfma_f32_32x32x16_bf16 v[96:111], v[72:75], a[248:251], v[96:111]\n"
"ds_read_b128 v[68:71], v1 offset:52224\n"
"buffer_load_dwordx4 v[228:231], v1, s[48:51], s21 offen sc1\n"
"v_mul_f32 v19, v16, v87\n"
"v_fmac_f32 v95, v15, v55\n"
"v_mfma_f32_32x32x16_bf16 v[96:111], v[76:79], a[252:255], v[96:111]\n"
"ds_read_b128 v[72:75], v1 offset:53248\n"
"v_exp_f32 v19, v19\n"
"v_mul_f32 v86, v86, v94\n"
"v_fma_f32 v18, v18, v17, v17\n"
"s_waitcnt lgkmcnt(3)\n"
"v_mfma_f32_32x32x16_bf16 v[112:127], v[56:59], v[128:131], 0\n"
"ds_read_b128 v[76:79], v1 offset:54272\n"
"buffer_load_dwordx4 v[232:235], v1, s[48:51], s22 offen sc1\n"
"v_rcp_f32 v18, v18\n"
"s_nop 0\n"
"v_mul_f32 v86, v86, v18\n"
"v_mfma_f32_32x32x16_bf16 v[112:127], v[60:63], v[132:135], v[112:127]\n"
"ds_read_b128 v[56:59], v1 offset:55296\n"
"v_mul_f32 v87, v87, v95\n"
"v_fma_f32 v19, v19, v17, v17\n"
"v_rcp_f32 v19, v19\n"
"s_nop 0\n"
"v_mul_f32 v87, v87, v19\n"
"s_waitcnt lgkmcnt(3)\n"
"v_mfma_f32_32x32x16_bf16 v[112:127], v[64:67], v[136:139], v[112:127]\n"
"ds_read_b128 v[60:63], v1 offset:56320\n"
"buffer_load_dwordx4 v[236:239], v1, s[48:51], s23 offen sc1\n"
"v_cvt_pk_bf16_f32 v35, v86, v87\n"
"buffer_store_dwordx4 v[32:35], v30, s[48:51], s39 offen sc1\n"
"ds_read_b128 v[40:43], v14 offset:640\n"
"v_mfma_f32_32x32x16_bf16 v[112:127], v[68:71], v[140:143], v[112:127]\n"
"ds_read_b128 v[64:67], v1 offset:57344\n"
"ds_read_b128 v[48:51], v14 offset:672\n"
"ds_read_b128 v[44:47], v14 offset:656\n"
"ds_read_b128 v[52:55], v14 offset:688\n"
"s_waitcnt lgkmcnt(4)\n"
"v_fmac_f32 v96, v15, v40\n"
"v_mul_f32 v18, v16, v96\n"
"v_mfma_f32_32x32x16_bf16 v[112:127], v[72:75], v[144:147], v[112:127]\n"
"ds_read_b128 v[68:71], v1 offset:58368\n"
"s_waitcnt lgkmcnt(3)\n"
"v_fmac_f32 v104, v15, v48\n"
"v_exp_f32 v18, v18\n"
"v_fmac_f32 v97, v15, v41\n"
"v_mfma_f32_32x32x16_bf16 v[112:127], v[76:79], v[148:151], v[112:127]\n"
"ds_read_b128 v[72:75], v1 offset:59392\n"
"v_mul_f32 v19, v16, v97\n"
"v_fmac_f32 v105, v15, v49\n"
"v_exp_f32 v19, v19\n"
"v_mul_f32 v96, v96, v104\n"
"v_mfma_f32_32x32x16_bf16 v[112:127], v[56:59], v[152:155], v[112:127]\n"
"ds_read_b128 v[76:79], v1 offset:60416\n"
"buffer_load_dwordx4 v[128:131], v1, s[48:51], s24 offen sc1\n"
"v_fma_f32 v18, v18, v17, v17\n"
"v_rcp_f32 v18, v18\n"
"v_mfma_f32_32x32x16_bf16 v[112:127], v[60:63], v[156:159], v[112:127]\n"
"ds_read_b128 v[56:59], v1 offset:61440\n"
"v_mul_f32 v96, v96, v18\n"
"v_fmac_f32 v98, v15, v42\n"
"v_mul_f32 v11, v16, v98\n"
"v_fmac_f32 v106, v15, v50\n"
"s_waitcnt lgkmcnt(3)\n"
"v_mfma_f32_32x32x16_bf16 v[112:127], v[64:67], v[160:163], v[112:127]\n"
"ds_read_b128 v[60:63], v1 offset:62464\n"
"buffer_load_dwordx4 v[132:135], v1, s[48:51], s25 offen sc1\n"
"v_exp_f32 v11, v11\n"
"v_mul_f32 v97, v97, v105\n"
"v_mfma_f32_32x32x16_bf16 v[112:127], v[68:71], v[164:167], v[112:127]\n"
"ds_read_b128 v[64:67], v1 offset:63488\n"
"v_fma_f32 v19, v19, v17, v17\n"
"v_rcp_f32 v19, v19\n"
"s_nop 0\n"
"v_mul_f32 v97, v97, v19\n"
"v_cvt_pk_bf16_f32 v36, v96, v97\n"
"s_waitcnt lgkmcnt(3)\n"
"v_mfma_f32_32x32x16_bf16 v[112:127], v[72:75], v[168:171], v[112:127]\n"
"ds_read_b128 v[68:71], v1 offset:64512\n"
"buffer_load_dwordx4 v[136:139], v1, s[48:51], s26 offen sc1\n"
"v_fmac_f32 v99, v15, v43\n"
"v_mul_f32 v18, v16, v99\n"
"v_fmac_f32 v107, v15, v51\n"
"v_mfma_f32_32x32x16_bf16 v[112:127], v[76:79], v[172:175], v[112:127]\n"
"ds_read_b128 v[72:75], v12\n"
"v_exp_f32 v18, v18\n"
"v_mul_f32 v98, v98, v106\n"
"v_fma_f32 v11, v11, v17, v17\n"
"s_waitcnt lgkmcnt(3)\n"
"v_mfma_f32_32x32x16_bf16 v[112:127], v[56:59], v[176:179], v[112:127]\n"
"ds_read_b128 v[76:79], v12 offset:1024\n"
"buffer_load_dwordx4 v[80:83], v1, s[48:51], s27 offen sc1\n"
"v_rcp_f32 v11, v11\n"
"s_nop 0\n"
"v_mul_f32 v98, v98, v11\n"
"v_mfma_f32_32x32x16_bf16 v[112:127], v[60:63], v[180:183], v[112:127]\n"
"ds_read_b128 v[56:59], v12 offset:2048\n"
"v_fmac_f32 v100, v15, v44\n"
"v_mul_f32 v19, v16, v100\n"
"v_fmac_f32 v108, v15, v52\n"
"v_exp_f32 v19, v19\n"
"s_waitcnt lgkmcnt(3)\n"
"v_mfma_f32_32x32x16_bf16 v[112:127], v[64:67], v[184:187], v[112:127]\n"
"ds_read_b128 v[60:63], v12 offset:3072\n"
"buffer_load_dwordx4 v[84:87], v1, s[48:51], s20 offen offset:1024 sc1\n"
"v_mul_f32 v99, v99, v107\n"
"v_fma_f32 v18, v18, v17, v17\n"
"v_mfma_f32_32x32x16_bf16 v[112:127], v[68:71], v[188:191], v[112:127]\n"
"ds_read_b128 v[64:67], v12 offset:4096\n"
"v_rcp_f32 v18, v18\n"
"s_nop 0\n"
"v_mul_f32 v99, v99, v18\n"
"v_cvt_pk_bf16_f32 v37, v98, v99\n"
"v_fmac_f32 v101, v15, v45\n"
"s_waitcnt lgkmcnt(3)\n"
"v_mfma_f32_32x32x16_bf16 v[112:127], v[72:75], v[192:195], v[112:127]\n"
"ds_read_b128 v[68:71], v12 offset:5120\n"
"buffer_load_dwordx4 v[88:91], v1, s[48:51], s21 offen offset:1024 sc1\n"
"v_mul_f32 v11, v16, v101\n"
"v_fmac_f32 v109, v15, v53\n"
"v_mfma_f32_32x32x16_bf16 v[112:127], v[76:79], v[196:199], v[112:127]\n"
"ds_read_b128 v[72:75], v12 offset:6144\n"
"v_exp_f32 v11, v11\n"
"v_mul_f32 v100, v100, v108\n"
"v_fma_f32 v19, v19, v17, v17\n"
"s_waitcnt lgkmcnt(3)\n"
"v_mfma_f32_32x32x16_bf16 v[112:127], v[56:59], v[200:203], v[112:127]\n"
"ds_read_b128 v[76:79], v12 offset:7168\n"
"buffer_load_dwordx4 v[92:95], v1, s[48:51], s22 offen offset:1024 sc1\n"
"v_rcp_f32 v19, v19\n"
"s_nop 0\n"
"v_mul_f32 v100, v100, v19\n"
"v_mfma_f32_32x32x16_bf16 v[112:127], v[60:63], v[204:207], v[112:127]\n"
"ds_read_b128 v[56:59], v12 offset:8192\n"
"v_fmac_f32 v102, v15, v46\n"
"v_mul_f32 v18, v16, v102\n"
"v_fmac_f32 v110, v15, v54\n"
"v_exp_f32 v18, v18\n"
"s_waitcnt lgkmcnt(3)\n"
"v_mfma_f32_32x32x16_bf16 v[112:127], v[64:67], v[208:211], v[112:127]\n"
"ds_read_b128 v[60:63], v12 offset:9216\n"
"buffer_load_dwordx4 v[140:143], v1, s[48:51], s23 offen offset:1024 sc1\n"
"v_mul_f32 v101, v101, v109\n"
"v_fma_f32 v11, v11, v17, v17\n"
"v_mfma_f32_32x32x16_bf16 v[112:127], v[68:71], v[212:215], v[112:127]\n"
"ds_read_b128 v[64:67], v12 offset:10240\n"
"v_rcp_f32 v11, v11\n"
"s_nop 0\n"
"v_mul_f32 v101, v101, v11\n"
"v_cvt_pk_bf16_f32 v38, v100, v101\n"
"v_fmac_f32 v103, v15, v47\n"
"s_waitcnt lgkmcnt(3)\n"
"v_mfma_f32_32x32x16_bf16 v[112:127], v[72:75], v[216:219], v[112:127]\n"
"ds_read_b128 v[68:71], v12 offset:11264\n"
"buffer_load_dwordx4 v[144:147], v1, s[48:51], s24 offen offset:1024 sc1\n"
"v_mul_f32 v19, v16, v103\n"
"v_fmac_f32 v111, v15, v55\n"
"v_mfma_f32_32x32x16_bf16 v[112:127], v[76:79], v[220:223], v[112:127]\n"
"ds_read_b128 v[72:75], v12 offset:12288\n"
"v_exp_f32 v19, v19\n"
"v_mul_f32 v102, v102, v110\n"
"v_fma_f32 v18, v18, v17, v17\n"
"s_waitcnt lgkmcnt(3)\n"
"v_mfma_f32_32x32x16_bf16 v[112:127], v[56:59], a[224:227], v[112:127]\n"
"ds_read_b128 v[76:79], v12 offset:13312\n"
"buffer_load_dwordx4 v[148:151], v1, s[48:51], s25 offen offset:1024 sc1\n"
"v_rcp_f32 v18, v18\n"
"s_nop 0\n"
"v_mul_f32 v102, v102, v18\n"
"v_mfma_f32_32x32x16_bf16 v[112:127], v[60:63], a[228:231], v[112:127]\n"
"ds_read_b128 v[56:59], v12 offset:14336\n"
"v_mul_f32 v103, v103, v111\n"
"v_fma_f32 v19, v19, v17, v17\n"
"v_rcp_f32 v19, v19\n"
"s_nop 0\n"
"v_mul_f32 v103, v103, v19\n"
"s_waitcnt lgkmcnt(3)\n"
"v_mfma_f32_32x32x16_bf16 v[112:127], v[64:67], a[232:235], v[112:127]\n"
"ds_read_b128 v[60:63], v12 offset:15360\n"
"buffer_load_dwordx4 v[152:155], v1, s[48:51], s26 offen offset:1024 sc1\n"
"v_cvt_pk_bf16_f32 v39, v102, v103\n"
"buffer_store_dwordx4 v[36:39], v30, s[48:51], s39 offen offset:1024 sc1\n"
"v_mfma_f32_32x32x16_bf16 v[112:127], v[68:71], a[236:239], v[112:127]\n"
"s_waitcnt lgkmcnt(2)\n"
"v_mfma_f32_32x32x16_bf16 v[112:127], v[72:75], a[240:243], v[112:127]\n"
"v_mfma_f32_32x32x16_bf16 v[112:127], v[76:79], a[244:247], v[112:127]\n"
"s_waitcnt lgkmcnt(0)\n"
"v_mfma_f32_32x32x16_bf16 v[112:127], v[56:59], a[248:251], v[112:127]\n"
"v_mfma_f32_32x32x16_bf16 v[112:127], v[60:63], a[252:255], v[112:127]\n"
"buffer_load_dwordx4 v[156:159], v1, s[48:51], s27 offen offset:1024 sc1\n"
"s_waitcnt vmcnt(17)\n"
"v_mfma_f32_32x32x16_bf16 v[64:79], a[68:71], v[224:227], 0\n"
"v_mfma_f32_32x32x16_bf16 v[240:255], a[64:67], v[224:227], 0\n"
"buffer_load_dwordx4 v[160:163], v1, s[48:51], s20 offen offset:2048 sc1\n"
"ds_read_b128 v[40:43], v14 offset:768\n"
"ds_read_b128 v[48:51], v14 offset:800\n"
"ds_read_b128 v[44:47], v14 offset:784\n"
"ds_read_b128 v[52:55], v14 offset:816\n"
"s_waitcnt vmcnt(17)\n"
"v_mfma_f32_32x32x16_bf16 a[240:255], a[68:71], v[228:231], 0\n"
"s_waitcnt lgkmcnt(3)\n"
"v_fmac_f32 v112, v15, v40\n"
"v_mul_f32 v18, v16, v112\n"
"s_waitcnt lgkmcnt(2)\n"
"v_fmac_f32 v120, v15, v48\n"
"v_exp_f32 v18, v18\n"
"v_mfma_f32_32x32x16_bf16 a[224:239], a[64:67], v[228:231], 0\n"
"buffer_load_dwordx4 v[164:167], v1, s[48:51], s21 offen offset:2048 sc1\n"
"v_fmac_f32 v113, v15, v41\n"
"v_mul_f32 v19, v16, v113\n"
"v_fmac_f32 v121, v15, v49\n"
"s_waitcnt vmcnt(17)\n"
"v_mfma_f32_32x32x16_bf16 v[64:79], a[76:79], v[232:235], v[64:79]\n"
"v_exp_f32 v19, v19\n"
"v_mul_f32 v112, v112, v120\n"
"v_fma_f32 v18, v18, v17, v17\n"
"v_mfma_f32_32x32x16_bf16 v[240:255], a[72:75], v[232:235], v[240:255]\n"
"buffer_load_dwordx4 v[168:171], v1, s[48:51], s22 offen offset:2048 sc1\n"
"v_rcp_f32 v18, v18\n"
"s_nop 0\n"
"v_mul_f32 v112, v112, v18\n"
"v_fmac_f32 v114, v15, v42\n"
"s_waitcnt vmcnt(17)\n"
"v_mfma_f32_32x32x16_bf16 a[240:255], a[76:79], v[236:239], a[240:255]\n"
"v_mul_f32 v11, v16, v114\n"
"v_fmac_f32 v122, v15, v50\n"
"v_exp_f32 v11, v11\n"
"v_mul_f32 v113, v113, v121\n"
"v_mfma_f32_32x32x16_bf16 a[224:239], a[72:75], v[236:239], a[224:239]\n"
"buffer_load_dwordx4 v[172:175], v1, s[48:51], s23 offen offset:2048 sc1\n"
"v_fma_f32 v19, v19, v17, v17\n"
"v_rcp_f32 v19, v19\n"
"s_nop 0\n"
"v_mul_f32 v113, v113, v19\n"
"s_waitcnt vmcnt(16)\n"
"v_mfma_f32_32x32x16_bf16 v[64:79], a[84:87], v[128:131], v[64:79]\n"
"v_cvt_pk_bf16_f32 v32, v112, v113\n"
"v_fmac_f32 v115, v15, v43\n"
"v_mul_f32 v18, v16, v115\n"
"v_fmac_f32 v123, v15, v51\n"
"v_mfma_f32_32x32x16_bf16 v[240:255], a[80:83], v[128:131], v[240:255]\n"
"buffer_load_dwordx4 v[176:179], v1, s[48:51], s24 offen offset:2048 sc1\n"
"v_exp_f32 v18, v18\n"
"v_mul_f32 v114, v114, v122\n"
"v_fma_f32 v11, v11, v17, v17\n"
"s_waitcnt vmcnt(16)\n"
"v_mfma_f32_32x32x16_bf16 a[240:255], a[84:87], v[132:135], a[240:255]\n"
"v_rcp_f32 v11, v11\n"
"s_nop 0\n"
"v_mul_f32 v114, v114, v11\n"
"s_waitcnt lgkmcnt(1)\n"
"v_fmac_f32 v116, v15, v44\n"
"v_mul_f32 v19, v16, v116\n"
"v_mfma_f32_32x32x16_bf16 a[224:239], a[80:83], v[132:135], a[224:239]\n"
"buffer_load_dwordx4 v[180:183], v1, s[48:51], s25 offen offset:2048 sc1\n"
"s_waitcnt lgkmcnt(0)\n"
"v_fmac_f32 v124, v15, v52\n"
"v_exp_f32 v19, v19\n"
"v_mul_f32 v115, v115, v123\n"
"s_waitcnt vmcnt(16)\n"
"v_mfma_f32_32x32x16_bf16 v[64:79], a[92:95], v[136:139], v[64:79]\n"
"v_fma_f32 v18, v18, v17, v17\n"
"v_rcp_f32 v18, v18\n"
"s_nop 0\n"
"v_mul_f32 v115, v115, v18\n"
"v_cvt_pk_bf16_f32 v33, v114, v115\n"
"v_mfma_f32_32x32x16_bf16 v[240:255], a[88:91], v[136:139], v[240:255]\n"
"buffer_load_dwordx4 v[184:187], v1, s[48:51], s26 offen offset:2048 sc1\n"
"v_fmac_f32 v117, v15, v45\n"
"v_mul_f32 v11, v16, v117\n"
"v_fmac_f32 v125, v15, v53\n"
"s_waitcnt vmcnt(16)\n"
"v_mfma_f32_32x32x16_bf16 a[240:255], a[92:95], v[80:83], a[240:255]\n"
"v_exp_f32 v11, v11\n"
"v_mul_f32 v116, v116, v124\n"
"v_fma_f32 v19, v19, v17, v17\n"
"v_mfma_f32_32x32x16_bf16 a[224:239], a[88:91], v[80:83], a[224:239]\n"
"buffer_load_dwordx4 v[188:191], v1, s[48:51], s27 offen offset:2048 sc1\n"
"v_rcp_f32 v19, v19\n"
"s_nop 0\n"
"v_mul_f32 v116, v116, v19\n"
"v_fmac_f32 v118, v15, v46\n"
"s_waitcnt vmcnt(16)\n"
"v_mfma_f32_32x32x16_bf16 v[64:79], a[100:103], v[84:87], v[64:79]\n"
"v_mul_f32 v18, v16, v118\n"
"v_fmac_f32 v126, v15, v54\n"
"v_exp_f32 v18, v18\n"
"v_mul_f32 v117, v117, v125\n"
"v_mfma_f32_32x32x16_bf16 v[240:255], a[96:99], v[84:87], v[240:255]\n"
"buffer_load_dwordx4 v[192:195], v1, s[48:51], s20 offen offset:3072 sc1\n"
"v_fma_f32 v11, v11, v17, v17\n"
"v_rcp_f32 v11, v11\n"
"s_nop 0\n"
"v_mul_f32 v117, v117, v11\n"
"s_waitcnt vmcnt(16)\n"
"v_mfma_f32_32x32x16_bf16 a[240:255], a[100:103], v[88:91], a[240:255]\n"
"v_cvt_pk_bf16_f32 v34, v116, v117\n"
"v_fmac_f32 v119, v15, v47\n"
"v_mul_f32 v19, v16, v119\n"
"v_fmac_f32 v127, v15, v55\n"
"v_mfma_f32_32x32x16_bf16 a[224:239], a[96:99], v[88:91], a[224:239]\n"
"buffer_load_dwordx4 v[196:199], v1, s[48:51], s21 offen offset:3072 sc1\n"
"v_exp_f32 v19, v19\n"
"v_mul_f32 v118, v118, v126\n"
"v_fma_f32 v18, v18, v17, v17\n"
"s_waitcnt vmcnt(16)\n"
"v_mfma_f32_32x32x16_bf16 v[64:79], a[108:111], v[92:95], v[64:79]\n"
"v_rcp_f32 v18, v18\n"
"s_nop 0\n"
"v_mul_f32 v118, v118, v18\n"
"v_mul_f32 v119, v119, v127\n"
"v_fma_f32 v19, v19, v17, v17\n"
"v_mfma_f32_32x32x16_bf16 v[240:255], a[104:107], v[92:95], v[240:255]\n"
"buffer_load_dwordx4 v[200:203], v1, s[48:51], s22 offen offset:3072 sc1\n"
"v_rcp_f32 v19, v19\n"
"s_nop 0\n"
"v_mul_f32 v119, v119, v19\n"
"v_cvt_pk_bf16_f32 v35, v118, v119\n"
"s_waitcnt vmcnt(16)\n"
"v_mfma_f32_32x32x16_bf16 a[240:255], a[108:111], v[140:143], a[240:255]\n"
"buffer_store_dwordx4 v[32:35], v30, s[48:51], s39 offen offset:2048 sc1\n"
"v_mfma_f32_32x32x16_bf16 a[224:239], a[104:107], v[140:143], a[224:239]\n"
"s_waitcnt vmcnt(16)\n"
"v_mfma_f32_32x32x16_bf16 v[64:79], a[116:119], v[144:147], v[64:79]\n"
"v_mfma_f32_32x32x16_bf16 v[240:255], a[112:115], v[144:147], v[240:255]\n"
"buffer_load_dwordx4 v[204:207], v1, s[48:51], s23 offen offset:3072 sc1\n"
"s_waitcnt vmcnt(16)\n"
"v_mfma_f32_32x32x16_bf16 a[240:255], a[116:119], v[148:151], a[240:255]\n"
"v_mfma_f32_32x32x16_bf16 a[224:239], a[112:115], v[148:151], a[224:239]\n"
"s_waitcnt vmcnt(15)\n"
"v_mfma_f32_32x32x16_bf16 v[64:79], a[124:127], v[152:155], v[64:79]\n"
"buffer_load_dwordx4 v[208:211], v1, s[48:51], s24 offen offset:3072 sc1\n"
"v_mfma_f32_32x32x16_bf16 v[240:255], a[120:123], v[152:155], v[240:255]\n"
"s_waitcnt vmcnt(13)\n"
"v_max3_u32 v8, v160, v161, v162\n"
"s_waitcnt vmcnt(12)\n"
"v_max3_u32 v8, v163, v164, v8\n"
"v_max3_u32 v8, v165, v166, v8\n"
"v_max_u32 v8, v167, v8\n"
"v_cmp_eq_u32 vcc, -1, v8\n"
"v_mfma_f32_32x32x16_bf16 a[240:255], a[124:127], v[156:159], a[240:255]\n"
"buffer_load_dwordx4 v[212:215], v1, s[48:51], s25 offen offset:3072 sc1\n"
"v_mfma_f32_32x32x16_bf16 a[224:239], a[120:123], v[156:159], a[224:239]\n"
"s_nop 1\n"
"s_cbranch_vccnz .Lsl63\n"
".Lbk64:\n"
"v_mfma_f32_32x32x16_bf16 v[64:79], a[132:135], v[160:163], v[64:79]\n"
"buffer_load_dwordx4 v[216:219], v1, s[48:51], s26 offen offset:3072 sc1\n"
"v_mfma_f32_32x32x16_bf16 v[240:255], a[128:131], v[160:163], v[240:255]\n"
"s_waitcnt vmcnt(13)\n"
"v_max3_u32 v8, v168, v169, v170\n"
"s_waitcnt vmcnt(12)\n"
"v_max3_u32 v8, v171, v172, v8\n"
"v_max3_u32 v8, v173, v174, v8\n"
"v_max_u32 v8, v175, v8\n"
"v_cmp_eq_u32 vcc, -1, v8\n"
"v_mfma_f32_32x32x16_bf16 a[240:255], a[132:135], v[164:167], a[240:255]\n"
"buffer_load_dwordx4 v[220:223], v1, s[48:51], s27 offen offset:3072 sc1\n"
"v_mfma_f32_32x32x16_bf16 a[224:239], a[128:131], v[164:167], a[224:239]\n"
"s_nop 1\n"
"s_cbranch_vccnz .Lsl65\n"
".Lbk66:\n"
"v_mfma_f32_32x32x16_bf16 v[64:79], a[140:143], v[168:171], v[64:79]\n"
"buffer_load_dwordx4 v[96:99], v30, s[48:51], s20 offen sc1\n"
"v_mfma_f32_32x32x16_bf16 v[240:255], a[136:139], v[168:171], v[240:255]\n"
"s_waitcnt vmcnt(13)\n"
"v_max3_u32 v8, v176, v177, v178\n"
"s_waitcnt vmcnt(12)\n"
"v_max3_u32 v8, v179, v180, v8\n"
"v_max3_u32 v8, v181, v182, v8\n"
"v_max_u32 v8, v183, v8\n"
"v_cmp_eq_u32 vcc, -1, v8\n"
"v_mfma_f32_32x32x16_bf16 a[240:255], a[140:143], v[172:175], a[240:255]\n"
"buffer_load_dwordx4 v[100:103], v30, s[48:51], s21 offen sc1\n"
"v_mfma_f32_32x32x16_bf16 a[224:239], a[136:139], v[172:175], a[224:239]\n"
"s_nop 1\n"
"s_cbranch_vccnz .Lsl67\n"
".Lbk68:\n"
"v_mfma_f32_32x32x16_bf16 v[64:79], a[148:151], v[176:179], v[64:79]\n"
"buffer_load_dwordx4 v[104:107], v30, s[48:51], s22 offen sc1\n"
"v_mfma_f32_32x32x16_bf16 v[240:255], a[144:147], v[176:179], v[240:255]\n"
"s_waitcnt vmcnt(13)\n"
"v_max3_u32 v8, v184, v185, v186\n"
"s_waitcnt vmcnt(12)\n"
"v_max3_u32 v8, v187, v188, v8\n"
"v_max3_u32 v8, v189, v190, v8\n"
"v_max_u32 v8, v191, v8\n"
"v_cmp_eq_u32 vcc, -1, v8\n"
"v_mfma_f32_32x32x16_bf16 a[240:255], a[148:151], v[180:183], a[240:255]\n"
"buffer_load_dwordx4 v[108:111], v30, s[48:51], s23 offen sc1\n"
"v_mfma_f32_32x32x16_bf16 a[224:239], a[144:147], v[180:183], a[224:239]\n"
"s_nop 1\n"
"s_cbranch_vccnz .Lsl69\n"
".Lbk70:\n"
"v_mfma_f32_32x32x16_bf16 v[64:79], a[156:159], v[184:187], v[64:79]\n"
"buffer_load_dwordx4 v[56:59], v30, s[48:51], s24 offen sc1\n"
"v_mfma_f32_32x32x16_bf16 v[240:255], a[152:155], v[184:187], v[240:255]\n"
"s_waitcnt vmcnt(13)\n"
"v_max3_u32 v8, v192, v193, v194\n"
"s_waitcnt vmcnt(12)\n"
"v_max3_u32 v8, v195, v196, v8\n"
"v_max3_u32 v8, v197, v198, v8\n"
"v_max_u32 v8, v199, v8\n"
"v_cmp_eq_u32 vcc, -1, v8\n"
"v_mfma_f32_32x32x16_bf16 a[240:255], a[156:159], v[188:191], a[240:255]\n"
"buffer_load_dwordx4 v[60:63], v30, s[48:51], s25 offen sc1\n"
"v_mfma_f32_32x32x16_bf16 a[224:239], a[152:155], v[188:191], a[224:239]\n"
"s_nop 1\n"
"s_cbranch_vccnz .Lsl71\n"
".Lbk72:\n"
"v_mfma_f32_32x32x16_bf16 v[64:79], a[164:167], v[192:195], v[64:79]\n"
"buffer_load_dwordx4 v[224:227], v30, s[48:51], s26 offen sc1\n"
"v_mfma_f32_32x32x16_bf16 v[240:255], a[160:163], v[192:195], v[240:255]\n"
"s_waitcnt vmcnt(13)\n"
"v_max3_u32 v8, v200, v201, v202\n"
"s_waitcnt vmcnt(11)\n"
"v_max3_u32 v8, v203, v204, v8\n"
"v_max3_u32 v8, v205, v206, v8\n"
"v_max_u32 v8, v207, v8\n"
"v_cmp_eq_u32 vcc, -1, v8\n"
"v_mfma_f32_32x32x16_bf16 a[240:255], a[164:167], v[196:199], a[240:255]\n"
"buffer_load_dwordx4 v[228:231], v30, s[48:51], s27 offen sc1\n"
"v_mfma_f32_32x32x16_bf16 a[224:239], a[160:163], v[196:199], a[224:239]\n"
"s_nop 1\n"
"s_cbranch_vccnz .Lsl73\n"
".Lbk74:\n"
"v_mfma_f32_32x32x16_bf16 v[64:79], a[172:175], v[200:203], v[64:79]\n"
"buffer_load_dwordx4 v[232:235], v30, s[48:51], s20 offen offset:1024 sc1\n"
"v_mfma_f32_32x32x16_bf16 v[240:255], a[168:171], v[200:203], v[240:255]\n"
"s_waitcnt vmcnt(12)\n"
"v_max3_u32 v8, v208, v209, v210\n"
"s_waitcnt vmcnt(11)\n"
"v_max3_u32 v8, v211, v212, v8\n"
"v_max3_u32 v8, v213, v214, v8\n"
"v_max_u32 v8, v215, v8\n"
"v_cmp_eq_u32 vcc, -1, v8\n"
"v_mfma_f32_32x32x16_bf16 a[240:255], a[172:175], v[204:207], a[240:255]\n"
"buffer_load_dwordx4 v[236:239], v30, s[48:51], s21 offen offset:1024 sc1\n"
"v_mfma_f32_32x32x16_bf16 a[224:239], a[168:171], v[204:207], a[224:239]\n"
"s_nop 1\n"
"s_cbranch_vccnz .Lsl75\n"
".Lbk76:\n"
"v_mfma_f32_32x32x16_bf16 v[64:79], a[180:183], v[208:211], v[64:79]\n"
"buffer_load_dwordx4 v[128:131], v30, s[48:51], s22 offen offset:1024 sc1\n"
"v_mfma_f32_32x32x16_bf16 v[240:255], a[176:179], v[208:211], v[240:255]\n"
"s_waitcnt vmcnt(12)\n"
"v_max3_u32 v8, v216, v217, v218\n"
"s_waitcnt vmcnt(11)\n"
"v_max3_u32 v8, v219, v220, v8\n"
"v_max3_u32 v8, v221, v222, v8\n"
"v_max_u32 v8, v223, v8\n"
"v_cmp_eq_u32 vcc, -1, v8\n"
"v_mfma_f32_32x32x16_bf16 a[240:255], a[180:183], v[212:215], a[240:255]\n"
"buffer_load_dwordx4 v[132:135], v30, s[48:51], s23 offen offset:1024 sc1\n"
"v_mfma_f32_32x32x16_bf16 a[224:239], a[176:179], v[212:215], a[224:239]\n"
"s_nop 1\n"
"s_cbranch_vccnz .Lsl77\n"
".Lbk78:\n"
"v_mfma_f32_32x32x16_bf16 v[64:79], a[188:191], v[216:219], v[64:79]\n"
"v_mfma_f32_32x32x16_bf16 v[240:255], a[184:187], v[216:219], v[240:255]\n"
"s_waitcnt vmcnt(11)\n"
"v_max3_u32 v8, v96, v97, v98\n"
"s_waitcnt vmcnt(10)\n"
"v_max3_u32 v8, v99, v100, v8\n"
"v_max3_u32 v8, v101, v102, v8\n"
"v_max_u32 v8, v103, v8\n"
"v_cmp_eq_u32 vcc, -1, v8\n"
"buffer_load_dwordx4 v[136:139], v30, s[48:51], s24 offen offset:1024 sc1\n"
"v_mfma_f32_32x32x16_bf16 a[240:255], a[188:191], v[220:223], a[240:255]\n"
"buffer_load_dwordx4 v[80:83], v30, s[48:51], s25 offen offset:1024 sc1\n"
"v_mfma_f32_32x32x16_bf16 a[224:239], a[184:187], v[220:223], a[224:239]\n"
"s_nop 0\n"
"s_cbranch_vccnz .Lsl79\n"
".Lbk80:\n"
"v_mfma_f32_32x32x16_bf16 v[64:79], a[196:199], v[96:99], v[64:79]\n"
"v_mfma_f32_32x32x16_bf16 v[240:255], a[192:195], v[96:99], v[240:255]\n"
"s_waitcnt vmcnt(11)\n"
"v_max3_u32 v8, v104, v105, v106\n"
"s_waitcnt vmcnt(10)\n"
"v_max3_u32 v8, v107, v108, v8\n"
"v_max3_u32 v8, v109, v110, v8\n"
"v_max_u32 v8, v111, v8\n"
"v_cmp_eq_u32 vcc, -1, v8\n"
"buffer_load_dwordx4 v[84:87], v30, s[48:51], s26 offen offset:1024 sc1\n"
"v_mfma_f32_32x32x16_bf16 a[240:255], a[196:199], v[100:103], a[240:255]\n"
"buffer_load_dwordx4 v[88:91], v30, s[48:51], s27 offen offset:1024 sc1\n"
"v_mfma_f32_32x32x16_bf16 a[224:239], a[192:195], v[100:103], a[224:239]\n"
"s_nop 0\n"
"s_cbranch_vccnz .Lsl81\n"
".Lbk82:\n"
"v_mfma_f32_32x32x16_bf16 v[64:79], a[204:207], v[104:107], v[64:79]\n"
"v_mfma_f32_32x32x16_bf16 v[240:255], a[200:203], v[104:107], v[240:255]\n"
"s_waitcnt vmcnt(11)\n"
"v_max3_u32 v8, v56, v57, v58\n"
"s_waitcnt vmcnt(10)\n"
"v_max3_u32 v8, v59, v60, v8\n"
"v_max3_u32 v8, v61, v62, v8\n"
"v_max_u32 v8, v63, v8\n"
"v_cmp_eq_u32 vcc, -1, v8\n"
"buffer_load_dwordx4 v[92:95], v30, s[48:51], s21 offen offset:2048 sc1\n"
"v_mfma_f32_32x32x16_bf16 a[240:255], a[204:207], v[108:111], a[240:255]\n"
"buffer_load_dwordx4 v[140:143], v30, s[48:51], s23 offen offset:2048 sc1\n"
"v_mfma_f32_32x32x16_bf16 a[224:239], a[200:203], v[108:111], a[224:239]\n"
"s_nop 0\n"
"s_cbranch_vccnz .Lsl83\n"
".Lbk84:\n"
"v_mfma_f32_32x32x16_bf16 v[64:79], a[212:215], v[56:59], v[64:79]\n"
"v_mfma_f32_32x32x16_bf16 v[240:255], a[208:211], v[56:59], v[240:255]\n"
"s_waitcnt vmcnt(11)\n"
"v_max3_u32 v8, v224, v225, v226\n"
"s_waitcnt vmcnt(10)\n"
"v_max3_u32 v8, v227, v228, v8\n"
"v_max3_u32 v8, v229, v230, v8\n"
"v_max_u32 v8, v231, v8\n"
"v_cmp_eq_u32 vcc, -1, v8\n"
"buffer_load_dwordx4 v[144:147], v30, s[48:51], s25 offen offset:2048 sc1\n"
"v_mfma_f32_32x32x16_bf16 a[240:255], a[212:215], v[60:63], a[240:255]\n"
"buffer_load_dwordx4 v[148:151], v30, s[48:51], s27 offen offset:2048 sc1\n"
"v_mfma_f32_32x32x16_bf16 a[224:239], a[208:211], v[60:63], a[224:239]\n"
"s_nop 0\n"
"s_cbranch_vccnz .Lsl85\n"
".Lbk86:\n"
"v_mfma_f32_32x32x16_bf16 v[64:79], a[220:223], v[224:227], v[64:79]\n"
"v_lshlrev_b32 v112, 16, v20\n"
"v_and_b32 v113, 0xffff0000, v20\n"
"v_lshlrev_b32 v114, 16, v21\n"
"v_and_b32 v115, 0xffff0000, v21\n"
"v_lshlrev_b32 v116, 16, v22\n"
"v_mfma_f32_32x32x16_bf16 v[240:255], a[216:219], v[224:227], v[240:255]\n"
"s_waitcnt vmcnt(11)\n"
"v_max3_u32 v8, v232, v233, v234\n"
"s_waitcnt vmcnt(10)\n"
"v_max3_u32 v8, v235, v236, v8\n"
"v_max3_u32 v8, v237, v238, v8\n"
"v_max_u32 v8, v239, v8\n"
"v_cmp_eq_u32 vcc, -1, v8\n"
"buffer_load_dwordx4 v[152:155], v30, s[48:51], s20 offen offset:2048 sc1\n"
"v_mfma_f32_32x32x16_bf16 a[240:255], a[220:223], v[228:231], a[240:255]\n"
"buffer_load_dwordx4 v[156:159], v30, s[48:51], s22 offen offset:2048 sc1\n"
"v_and_b32 v117, 0xffff0000, v22\n"
"v_lshlrev_b32 v118, 16, v23\n"
"v_and_b32 v119, 0xffff0000, v23\n"
"v_lshlrev_b32 v120, 16, v24\n"
"v_mfma_f32_32x32x16_bf16 a[224:239], a[216:219], v[228:231], a[224:239]\n"
"s_cbranch_vccnz .Lsl87\n"
".Lbk88:\n"
"v_and_b32 v121, 0xffff0000, v24\n"
"v_lshlrev_b32 v122, 16, v25\n"
"v_and_b32 v123, 0xffff0000, v25\n"
"v_lshlrev_b32 v124, 16, v26\n"
"v_mfma_f32_32x32x16_bf16 v[64:79], a[4:7], v[232:235], v[64:79]\n"
"v_and_b32 v125, 0xffff0000, v26\n"
"v_lshlrev_b32 v126, 16, v27\n"
"v_and_b32 v127, 0xffff0000, v27\n"
"v_mfma_f32_32x32x16_bf16 v[240:255], a[0:3], v[232:235], v[240:255]\n"
"s_waitcnt vmcnt(11)\n"
"v_max3_u32 v8, v128, v129, v130\n"
"s_waitcnt vmcnt(10)\n"
"v_max3_u32 v8, v131, v132, v8\n"
"v_max3_u32 v8, v133, v134, v8\n"
"v_max_u32 v8, v135, v8\n"
"v_cmp_eq_u32 vcc, -1, v8\n"
"buffer_load_dwordx4 v[160:163], v30, s[48:51], s24 offen offset:2048 sc1\n"
"v_mfma_f32_32x32x16_bf16 a[240:255], a[4:7], v[236:239], a[240:255]\n"
"buffer_load_dwordx4 v[164:167], v30, s[48:51], s26 offen offset:2048 sc1\n"
"v_mfma_f32_32x32x16_bf16 a[224:239], a[0:3], v[236:239], a[224:239]\n"
"s_nop 0\n"
"s_cbranch_vccnz .Lsl89\n"
".Lbk90:\n"
"v_mfma_f32_32x32x16_bf16 v[64:79], a[12:15], v[128:131], v[64:79]\n"
"v_mfma_f32_32x32x16_bf16 v[240:255], a[8:11], v[128:131], v[240:255]\n"
"s_waitcnt vmcnt(11)\n"
"v_max3_u32 v8, v136, v137, v138\n"
"s_waitcnt vmcnt(10)\n"
"v_max3_u32 v8, v139, v80, v8\n"
"v_max3_u32 v8, v81, v82, v8\n"
"v_max_u32 v8, v83, v8\n"
"v_cmp_eq_u32 vcc, -1, v8\n"
"v_mfma_f32_32x32x16_bf16 a[240:255], a[12:15], v[132:135], a[240:255]\n"
"v_mfma_f32_32x32x16_bf16 a[224:239], a[8:11], v[132:135], a[224:239]\n"
"s_nop 2\n"
"s_cbranch_vccnz .Lsl91\n"
".Lbk92:\n"
"v_mfma_f32_32x32x16_bf16 v[64:79], a[20:23], v[136:139], v[64:79]\n"
"v_mfma_f32_32x32x16_bf16 v[240:255], a[16:19], v[136:139], v[240:255]\n"
"s_waitcnt vmcnt(9)\n"
"v_max3_u32 v8, v84, v85, v86\n"
"s_waitcnt vmcnt(8)\n"
"v_max3_u32 v8, v87, v88, v8\n"
"v_max3_u32 v8, v89, v90, v8\n"
"v_max_u32 v8, v91, v8\n"
"v_cmp_eq_u32 vcc, -1, v8\n"
"v_mfma_f32_32x32x16_bf16 a[240:255], a[20:23], v[80:83], a[240:255]\n"
"v_mfma_f32_32x32x16_bf16 a[224:239], a[16:19], v[80:83], a[224:239]\n"
"s_nop 2\n"
"s_cbranch_vccnz .Lsl93\n"
".Lbk94:\n"
"v_mfma_f32_32x32x16_bf16 v[64:79], a[28:31], v[84:87], v[64:79]\n"
"v_mfma_f32_32x32x16_bf16 v[240:255], a[24:27], v[84:87], v[240:255]\n"
"s_waitcnt vmcnt(7)\n"
"v_max3_u32 v8, v92, v93, v94\n"
"s_waitcnt vmcnt(6)\n"
"v_max3_u32 v8, v95, v140, v8\n"
"v_max3_u32 v8, v141, v142, v8\n"
"s_waitcnt vmcnt(5)\n"
"v_max3_u32 v8, v143, v144, v8\n"
"v_max3_u32 v8, v145, v146, v8\n"
"v_mfma_f32_32x32x16_bf16 a[240:255], a[28:31], v[88:91], a[240:255]\n"
"s_waitcnt vmcnt(4)\n"
"v_max3_u32 v8, v147, v148, v8\n"
"v_max3_u32 v8, v149, v150, v8\n"
"v_max_u32 v8, v151, v8\n"
"v_cmp_eq_u32 vcc, -1, v8\n"
"v_mfma_f32_32x32x16_bf16 a[224:239], a[24:27], v[88:91], a[224:239]\n"
"s_nop 3\n"
"s_cbranch_vccnz .Lsl95\n"
".Lbk96:\n"
"v_mfma_f32_32x32x16_bf16 a[240:255], a[36:39], v[92:95], a[240:255]\n"
"v_mfma_f32_32x32x16_bf16 a[240:255], a[44:47], v[140:143], a[240:255]\n"
"v_mfma_f32_32x32x16_bf16 a[240:255], a[52:55], v[144:147], a[240:255]\n"
"v_mfma_f32_32x32x16_bf16 a[240:255], a[60:63], v[148:151], a[240:255]\n"
"v_mfma_f32_32x32x16_bf16 a[224:239], a[32:35], v[92:95], a[224:239]\n"
"v_mfma_f32_32x32x16_bf16 a[224:239], a[40:43], v[140:143], a[224:239]\n"
"s_waitcnt vmcnt(3)\n"
"v_max3_u32 v8, v152, v153, v154\n"
"s_waitcnt vmcnt(2)\n"
"v_max3_u32 v8, v155, v156, v8\n"
"v_max3_u32 v8, v157, v158, v8\n"
"s_waitcnt vmcnt(1)\n"
"v_max3_u32 v8, v159, v160, v8\n"
"v_max3_u32 v8, v161, v162, v8\n"
"v_mfma_f32_32x32x16_bf16 a[224:239], a[48:51], v[144:147], a[224:239]\n"
"s_waitcnt vmcnt(0)\n"
"v_max3_u32 v8, v163, v164, v8\n"
"v_max3_u32 v8, v165, v166, v8\n"
"v_max_u32 v8, v167, v8\n"
"v_cmp_eq_u32 vcc, -1, v8\n"
"ds_write_b128 v4, a[240:243]\n"
"v_mfma_f32_32x32x16_bf16 a[224:239], a[56:59], v[148:151], a[224:239]\n"
"s_nop 2\n"
"s_cbranch_vccnz .Lsl97\n"
".Lbk98:\n"
"ds_write_b128 v4, a[244:247] offset:1024\n"
"ds_write_b128 v4, a[248:251] offset:2048\n"
"ds_write_b128 v4, a[252:255] offset:3072\n"
"v_mfma_f32_32x32x16_bf16 v[64:79], a[36:39], v[152:155], v[64:79]\n"
"v_mfma_f32_32x32x16_bf16 v[64:79], a[44:47], v[156:159], v[64:79]\n"
"v_mfma_f32_32x32x16_bf16 v[64:79], a[52:55], v[160:163], v[64:79]\n"
"s_nop 1\n"
"ds_write_b128 v3, a[224:227]\n"
"ds_write_b128 v3, a[228:231] offset:1024\n"
"ds_write_b128 v3, a[232:235] offset:2048\n"
"ds_write_b128 v3, a[236:239] offset:3072\n"
"v_mfma_f32_32x32x16_bf16 v[64:79], a[60:63], v[164:167], v[64:79]\n"
"v_mfma_f32_32x32x16_bf16 v[240:255], a[32:35], v[152:155], v[240:255]\n"
"v_mfma_f32_32x32x16_bf16 v[240:255], a[40:43], v[156:159], v[240:255]\n"
"v_mfma_f32_32x32x16_bf16 v[240:255], a[48:51], v[160:163], v[240:255]\n"
"v_mfma_f32_32x32x16_bf16 v[240:255], a[56:59], v[164:167], v[240:255]\n"
"s_nop 7\n"
"ds_write_b128 v2, v[64:67]\n"
"ds_write_b128 v2, v[68:71] offset:1024\n"
"ds_write_b128 v2, v[72:75] offset:2048\n"
"ds_write_b128 v2, v[76:79] offset:3072\n"
"s_waitcnt lgkmcnt(0)\n"
"s_barrier\n"
"ds_read_b128 v[128:131], v5\n"
"ds_read_b128 v[132:135], v5 offset:1024\n"
"ds_read_b128 v[136:139], v5 offset:2048\n"
"ds_read_b128 v[140:143], v5 offset:3072\n"
"ds_read_b128 v[144:147], v5 offset:4096\n"
"ds_read_b128 v[148:151], v5 offset:5120\n"
"ds_read_b128 v[152:155], v5 offset:6144\n"
"ds_read_b128 v[156:159], v5 offset:7168\n"
"ds_read_b128 v[160:163], v5 offset:8192\n"
"ds_read_b128 v[164:167], v5 offset:9216\n"
"ds_read_b128 v[168:171], v5 offset:10240\n"
"ds_read_b128 v[172:175], v5 offset:11264\n"
"v_pk_add_f32 v[240:241], v[240:241], v[112:113]\n"
"v_pk_add_f32 v[242:243], v[242:243], v[114:115]\n"
"v_pk_add_f32 v[244:245], v[244:245], v[116:117]\n"
"v_pk_add_f32 v[246:247], v[246:247], v[118:119]\n"
"v_pk_add_f32 v[248:249], v[248:249], v[120:121]\n"
"v_pk_add_f32 v[250:251], v[250:251], v[122:123]\n"
"v_pk_add_f32 v[252:253], v[252:253], v[124:125]\n"
"v_pk_add_f32 v[254:255], v[254:255], v[126:127]\n"
"s_waitcnt lgkmcnt(11)\n"
"v_pk_add_f32 v[240:241], v[240:241], v[128:129]\n"
"v_pk_add_f32 v[242:243], v[242:243], v[130:131]\n"
"s_waitcnt lgkmcnt(10)\n"
"v_pk_add_f32 v[244:245], v[244:245], v[132:133]\n"
"v_pk_add_f32 v[246:247], v[246:247], v[134:135]\n"
"s_waitcnt lgkmcnt(9)\n"
"v_pk_add_f32 v[248:249], v[248:249], v[136:137]\n"
"v_pk_add_f32 v[250:251], v[250:251], v[138:139]\n"
"s_waitcnt lgkmcnt(8)\n"
"v_pk_add_f32 v[252:253], v[252:253], v[140:141]\n"
"v_pk_add_f32 v[254:255], v[254:255], v[142:143]\n"
"s_waitcnt lgkmcnt(7)\n"
"v_pk_add_f32 v[240:241], v[240:241], v[144:145]\n"
"v_pk_add_f32 v[242:243], v[242:243], v[146:147]\n"
"s_waitcnt lgkmcnt(6)\n"
"v_pk_add_f32 v[244:245], v[244:245], v[148:149]\n"
"v_pk_add_f32 v[246:247], v[246:247], v[150:151]\n"
"s_waitcnt lgkmcnt(5)\n"
"v_pk_add_f32 v[248:249], v[248:249], v[152:153]\n"
"v_pk_add_f32 v[250:251], v[250:251], v[154:155]\n"
"s_waitcnt lgkmcnt(4)\n"
"v_pk_add_f32 v[252:253], v[252:253], v[156:157]\n"
"v_pk_add_f32 v[254:255], v[254:255], v[158:159]\n"
"s_waitcnt lgkmcnt(3)\n"
"v_pk_add_f32 v[240:241], v[240:241], v[160:161]\n"
"v_pk_add_f32 v[242:243], v[242:243], v[162:163]\n"
"s_waitcnt lgkmcnt(2)\n"
"v_pk_add_f32 v[244:245], v[244:245], v[164:165]\n"
"v_pk_add_f32 v[246:247], v[246:247], v[166:167]\n"
"s_waitcnt lgkmcnt(1)\n"
"v_pk_add_f32 v[248:249], v[248:249], v[168:169]\n"
"v_pk_add_f32 v[250:251], v[250:251], v[170:171]\n"
"s_waitcnt lgkmcnt(0)\n"
"v_pk_add_f32 v[252:253], v[252:253], v[172:173]\n"
"v_pk_add_f32 v[254:255], v[254:255], v[174:175]\n"
"v_cvt_pk_bf16_f32 v20, v240, v241\n"
"v_cvt_pk_bf16_f32 v21, v242, v243\n"
"v_cvt_pk_bf16_f32 v22, v244, v245\n"
"v_cvt_pk_bf16_f32 v23, v246, v247\n"
"v_cvt_pk_bf16_f32 v24, v248, v249\n"
"v_cvt_pk_bf16_f32 v25, v250, v251\n"
"v_cvt_pk_bf16_f32 v26, v252, v253\n"
"v_cvt_pk_bf16_f32 v27, v254, v255\n"
"buffer_store_dwordx4 v[20:23], v0, s[52:55], s28 offen\n"
"buffer_store_dwordx4 v[24:27], v0, s[52:55], s28 offen offset:32\n"
"s_or_b32 s77, %1, s68\n"
"s_cmp_lg_u32 s77, 0\n"
"s_cbranch_scc1 .Lep99\n"
"s_mov_b64 exec, 1\n"
"buffer_store_dwordx2 v[28:29], off, s[48:51], 0 sc1\n"
"s_mov_b64 exec, -1\n"
".Lep99:\n"
"s_branch .Lcode1\n"
".Lsl10:\n"
"s_mov_b32 s83, 50000\n"
".Lsp100:\n"
"s_sleep 4\n"
"buffer_load_dwordx4 v[68:71], v1, s[48:51], s22 offen sc1\n"
"buffer_load_dwordx4 v[76:79], v1, s[48:51], s22 offen offset:1024 sc1\n"
"buffer_load_dwordx4 v[84:87], v1, s[48:51], s22 offen offset:2048 sc1\n"
"buffer_load_dwordx4 v[92:95], v1, s[48:51], s22 offen offset:3072 sc1\n"
"buffer_load_dwordx4 v[100:103], v1, s[48:51], s23 offen sc1\n"
"buffer_load_dwordx4 v[108:111], v1, s[48:51], s23 offen offset:1024 sc1\n"
"buffer_load_dwordx4 v[116:119], v1, s[48:51], s23 offen offset:2048 sc1\n"
"buffer_load_dwordx4 v[124:127], v1, s[48:51], s23 offen offset:3072 sc1\n"
"buffer_load_dwordx4 v[64:67], v1, s[48:51], s20 offen sc1\n"
"buffer_load_dwordx4 v[72:75], v1, s[48:51], s20 offen offset:1024 sc1\n"
"s_waitcnt vmcnt(0)\n"
"v_max3_u32 v36, v68, v69, v70\n"
"v_max3_u32 v36, v71, v76, v36\n"
"v_max3_u32 v36, v77, v78, v36\n"
"v_max_u32 v36, v79, v36\n"
"v_cmp_eq_u32 vcc, -1, v36\n"
"s_sub_u32 s83, s83, 1\n"
"s_cbranch_scc1 .Lbk11\n"
"s_nop 2\n"
"s_cbranch_vccnz .Lsp100\n"
"s_branch .Lbk11\n"
".Lsl12:\n"
"s_mov_b32 s83, 50000\n"
".Lsp101:\n"
"s_sleep 4\n"
"buffer_load_dwordx4 v[84:87], v1, s[48:51], s22 offen offset:2048 sc1\n"
"buffer_load_dwordx4 v[92:95], v1, s[48:51], s22 offen offset:3072 sc1\n"
"buffer_load_dwordx4 v[100:103], v1, s[48:51], s23 offen sc1\n"
"buffer_load_dwordx4 v[108:111], v1, s[48:51], s23 offen offset:1024 sc1\n"
"buffer_load_dwordx4 v[116:119], v1, s[48:51], s23 offen offset:2048 sc1\n"
"buffer_load_dwordx4 v[124:127], v1, s[48:51], s23 offen offset:3072 sc1\n"
"buffer_load_dwordx4 v[64:67], v1, s[48:51], s20 offen sc1\n"
"buffer_load_dwordx4 v[72:75], v1, s[48:51], s20 offen offset:1024 sc1\n"
"buffer_load_dwordx4 v[80:83], v1, s[48:51], s20 offen offset:2048 sc1\n"
"buffer_load_dwordx4 v[88:91], v1, s[48:51], s20 offen offset:3072 sc1\n"
"buffer_load_dwordx4 v[96:99], v1, s[48:51], s21 offen sc1\n"
"s_waitcnt vmcnt(0)\n"
"v_max3_u32 v36, v84, v85, v86\n"
"v_max3_u32 v36, v87, v92, v36\n"
"v_max3_u32 v36, v93, v94, v36\n"
"v_max_u32 v36, v95, v36\n"
"v_cmp_eq_u32 vcc, -1, v36\n"
"s_sub_u32 s83, s83, 1\n"
"s_cbranch_scc1 .Lbk13\n"
"s_nop 2\n"
"s_cbranch_vccnz .Lsp101\n"
"s_branch .Lbk13\n"
".Lsl14:\n"
"s_mov_b32 s83, 50000\n"
".Lsp102:\n"
"s_sleep 4\n"
"buffer_load_dwordx4 v[100:103], v1, s[48:51], s23 offen sc1\n"
"buffer_load_dwordx4 v[108:111], v1, s[48:51], s23 offen offset:1024 sc1\n"
"buffer_load_dwordx4 v[116:119], v1, s[48:51], s23 offen offset:2048 sc1\n"
"buffer_load_dwordx4 v[124:127], v1, s[48:51], s23 offen offset:3072 sc1\n"
"buffer_load_dwordx4 v[64:67], v1, s[48:51], s20 offen sc1\n"
"buffer_load_dwordx4 v[72:75], v1, s[48:51], s20 offen offset:1024 sc1\n"
"buffer_load_dwordx4 v[80:83], v1, s[48:51], s20 offen offset:2048 sc1\n"
"buffer_load_dwordx4 v[88:91], v1, s[48:51], s20 offen offset:3072 sc1\n"
"buffer_load_dwordx4 v[96:99], v1, s[48:51], s21 offen sc1\n"
"buffer_load_dwordx4 v[104:107], v1, s[48:51], s21 offen offset:1024 sc1\n"
"buffer_load_dwordx4 v[112:115], v1, s[48:51], s21 offen offset:2048 sc1\n"
"buffer_load_dwordx4 v[120:123], v1, s[48:51], s21 offen offset:3072 sc1\n"
"s_waitcnt vmcnt(0)\n"
"v_max3_u32 v36, v100, v101, v102\n"
"v_max3_u32 v36, v103, v108, v36\n"
"v_max3_u32 v36, v109, v110, v36\n"
"v_max_u32 v36, v111, v36\n"
"v_cmp_eq_u32 vcc, -1, v36\n"
"s_sub_u32 s83, s83, 1\n"
"s_cbranch_scc1 .Lbk15\n"
"s_nop 2\n"
"s_cbranch_vccnz .Lsp102\n"
"s_branch .Lbk15\n"
".Lsl16:\n"
"s_mov_b32 s83, 50000\n"
".Lsp103:\n"
"s_sleep 4\n"
"buffer_load_dwordx4 v[116:119], v1, s[48:51], s23 offen offset:2048 sc1\n"
"buffer_load_dwordx4 v[124:127], v1, s[48:51], s23 offen offset:3072 sc1\n"
"buffer_load_dwordx4 v[64:67], v1, s[48:51], s20 offen sc1\n"
"buffer_load_dwordx4 v[72:75], v1, s[48:51], s20 offen offset:1024 sc1\n"
"buffer_load_dwordx4 v[80:83], v1, s[48:51], s20 offen offset:2048 sc1\n"
"buffer_load_dwordx4 v[88:91], v1, s[48:51], s20 offen offset:3072 sc1\n"
"buffer_load_dwordx4 v[96:99], v1, s[48:51], s21 offen sc1\n"
"buffer_load_dwordx4 v[104:107], v1, s[48:51], s21 offen offset:1024 sc1\n"
"buffer_load_dwordx4 v[112:115], v1, s[48:51], s21 offen offset:2048 sc1\n"
"buffer_load_dwordx4 v[120:123], v1, s[48:51], s21 offen offset:3072 sc1\n"
"s_waitcnt vmcnt(0)\n"
"v_max3_u32 v36, v116, v117, v118\n"
"v_max3_u32 v36, v119, v124, v36\n"
"v_max3_u32 v36, v125, v126, v36\n"
"v_max_u32 v36, v127, v36\n"
"v_cmp_eq_u32 vcc, -1, v36\n"
"s_sub_u32 s83, s83, 1\n"
"s_cbranch_scc1 .Lbk17\n"
"s_nop 2\n"
"s_cbranch_vccnz .Lsp103\n"
"s_branch .Lbk17\n"
".Lsl18:\n"
"s_mov_b32 s83, 50000\n"
".Lsp104:\n"
"s_sleep 4\n"
"buffer_load_dwordx4 v[64:67], v1, s[48:51], s20 offen sc1\n"
"buffer_load_dwordx4 v[72:75], v1, s[48:51], s20 offen offset:1024 sc1\n"
"buffer_load_dwordx4 v[80:83], v1, s[48:51], s20 offen offset:2048 sc1\n"
"buffer_load_dwordx4 v[88:91], v1, s[48:51], s20 offen offset:3072 sc1\n"
"buffer_load_dwordx4 v[96:99], v1, s[48:51], s21 offen sc1\n"
"buffer_load_dwordx4 v[104:107], v1, s[48:51], s21 offen offset:1024 sc1\n"
"buffer_load_dwordx4 v[112:115], v1, s[48:51], s21 offen offset:2048 sc1\n"
"buffer_load_dwordx4 v[120:123], v1, s[48:51], s21 offen offset:3072 sc1\n"
"s_waitcnt vmcnt(0)\n"
"v_max3_u32 v36, v64, v65, v66\n"
"v_max3_u32 v36, v67, v72, v36\n"
"v_max3_u32 v36, v73, v74, v36\n"
"v_max_u32 v36, v75, v36\n"
"v_cmp_eq_u32 vcc, -1, v36\n"
"s_sub_u32 s83, s83, 1\n"
"s_cbranch_scc1 .Lbk19\n"
"s_nop 2\n"
"s_cbranch_vccnz .Lsp104\n"
"s_branch .Lbk19\n"
".Lsl20:\n"
"s_mov_b32 s83, 50000\n"
".Lsp105:\n"
"s_sleep 4\n"
"buffer_load_dwordx4 v[80:83], v1, s[48:51], s20 offen offset:2048 sc1\n"
"buffer_load_dwordx4 v[88:91], v1, s[48:51], s20 offen offset:3072 sc1\n"
"buffer_load_dwordx4 v[96:99], v1, s[48:51], s21 offen sc1\n"
"buffer_load_dwordx4 v[104:107], v1, s[48:51], s21 offen offset:1024 sc1\n"
"buffer_load_dwordx4 v[112:115], v1, s[48:51], s21 offen offset:2048 sc1\n"
"buffer_load_dwordx4 v[120:123], v1, s[48:51], s21 offen offset:3072 sc1\n"
"s_waitcnt vmcnt(0)\n"
"v_max3_u32 v36, v80, v81, v82\n"
"v_max3_u32 v36, v83, v88, v36\n"
"v_max3_u32 v36, v89, v90, v36\n"
"v_max_u32 v36, v91, v36\n"
"v_cmp_eq_u32 vcc, -1, v36\n"
"s_sub_u32 s83, s83, 1\n"
"s_cbranch_scc1 .Lbk21\n"
"s_nop 2\n"
"s_cbranch_vccnz .Lsp105\n"
"s_branch .Lbk21\n"
".Lsl22:\n"
"s_mov_b32 s83, 50000\n"
".Lsp106:\n"
"s_sleep 4\n"
"buffer_load_dwordx4 v[96:99], v1, s[48:51], s21 offen sc1\n"
"buffer_load_dwordx4 v[104:107], v1, s[48:51], s21 offen offset:1024 sc1\n"
"buffer_load_dwordx4 v[112:115], v1, s[48:51], s21 offen offset:2048 sc1\n"
"buffer_load_dwordx4 v[120:123], v1, s[48:51], s21 offen offset:3072 sc1\n"
"s_waitcnt vmcnt(0)\n"
"v_max3_u32 v36, v96, v97, v98\n"
"v_max3_u32 v36, v99, v104, v36\n"
"v_max3_u32 v36, v105, v106, v36\n"
"v_max_u32 v36, v107, v36\n"
"v_cmp_eq_u32 vcc, -1, v36\n"
"s_sub_u32 s83, s83, 1\n"
"s_cbranch_scc1 .Lbk23\n"
"s_nop 2\n"
"s_cbranch_vccnz .Lsp106\n"
"s_branch .Lbk23\n"
".Lsl24:\n"
"s_mov_b32 s83, 50000\n"
".Lsp107:\n"
"s_sleep 4\n"
"buffer_load_dwordx4 v[112:115], v1, s[48:51], s21 offen offset:2048 sc1\n"
"buffer_load_dwordx4 v[120:123], v1, s[48:51], s21 offen offset:3072 sc1\n"
"s_waitcnt vmcnt(0)\n"
"v_max3_u32 v36, v112, v113, v114\n"
"v_max3_u32 v36, v115, v120, v36\n"
"v_max3_u32 v36, v121, v122, v36\n"
"v_max_u32 v36, v123, v36\n"
"v_cmp_eq_u32 vcc, -1, v36\n"
"s_sub_u32 s83, s83, 1\n"
"s_cbranch_scc1 .Lbk25\n"
"s_nop 2\n"
"s_cbranch_vccnz .Lsp107\n"
"s_branch .Lbk25\n"
".Lsl26:\n"
"s_mov_b32 s83, 50000\n"
".Lsp108:\n"
"s_sleep 4\n"
"s_mov_b32 s82, 0\n"
"buffer_load_dwordx4 v[128:131], v10, s[48:51], s82 offen sc1\n"
"buffer_load_dwordx4 v[132:135], v10, s[48:51], s82 offen offset:1024 sc1\n"
"buffer_load_dwordx4 v[136:139], v10, s[48:51], s82 offen offset:2048 sc1\n"
"buffer_load_dwordx4 v[140:143], v10, s[48:51], s82 offen offset:3072 sc1\n"
"s_mov_b32 s82, 4096\n"
"buffer_load_dwordx4 v[144:147], v10, s[48:51], s82 offen sc1\n"
"buffer_load_dwordx4 v[148:151], v10, s[48:51], s82 offen offset:1024 sc1\n"
"buffer_load_dwordx4 v[152:155], v10, s[48:51], s82 offen offset:2048 sc1\n"
"buffer_load_dwordx4 v[156:159], v10, s[48:51], s82 offen offset:3072 sc1\n"
"s_mov_b32 s82, 16384\n"
"buffer_load_dwordx4 v[160:163], v10, s[48:51], s82 offen sc1\n"
"buffer_load_dwordx4 v[164:167], v10, s[48:51], s82 offen offset:1024 sc1\n"
"buffer_load_dwordx4 v[168:171], v10, s[48:51], s82 offen offset:2048 sc1\n"
"buffer_load_dwordx4 v[172:175], v10, s[48:51], s82 offen offset:3072 sc1\n"
"s_mov_b32 s82, 20480\n"
"buffer_load_dwordx4 v[176:179], v10, s[48:51], s82 offen sc1\n"
"buffer_load_dwordx4 v[180:183], v10, s[48:51], s82 offen offset:1024 sc1\n"
"buffer_load_dwordx4 v[184:187], v10, s[48:51], s82 offen offset:2048 sc1\n"
"buffer_load_dwordx4 v[188:191], v10, s[48:51], s82 offen offset:3072 sc1\n"
"s_waitcnt vmcnt(0)\n"
"v_max3_u32 v8, v128, v129, v130\n"
"v_max3_u32 v8, v131, v132, v8\n"
"v_max3_u32 v8, v133, v134, v8\n"
"v_max_u32 v8, v135, v8\n"
"v_cmp_eq_u32 vcc, -1, v8\n"
"s_sub_u32 s83, s83, 1\n"
"s_cbranch_scc1 .Lbk27\n"
"s_nop 2\n"
"s_cbranch_vccnz .Lsp108\n"
"s_branch .Lbk27\n"
".Lsl28:\n"
"s_mov_b32 s83, 50000\n"
".Lsp109:\n"
"s_sleep 4\n"
"s_mov_b32 s82, 0\n"
"buffer_load_dwordx4 v[136:139], v10, s[48:51], s82 offen offset:2048 sc1\n"
"buffer_load_dwordx4 v[140:143], v10, s[48:51], s82 offen offset:3072 sc1\n"
"s_mov_b32 s82, 4096\n"
"buffer_load_dwordx4 v[144:147], v10, s[48:51], s82 offen sc1\n"
"buffer_load_dwordx4 v[148:151], v10, s[48:51], s82 offen offset:1024 sc1\n"
"buffer_load_dwordx4 v[152:155], v10, s[48:51], s82 offen offset:2048 sc1\n"
"buffer_load_dwordx4 v[156:159], v10, s[48:51], s82 offen offset:3072 sc1\n"
"s_mov_b32 s82, 16384\n"
"buffer_load_dwordx4 v[160:163], v10, s[48:51], s82 offen sc1\n"
"buffer_load_dwordx4 v[164:167], v10, s[48:51], s82 offen offset:1024 sc1\n"
"buffer_load_dwordx4 v[168:171], v10, s[48:51], s82 offen offset:2048 sc1\n"
"buffer_load_dwordx4 v[172:175], v10, s[48:51], s82 offen offset:3072 sc1\n"
"s_mov_b32 s82, 20480\n"
"buffer_load_dwordx4 v[176:179], v10, s[48:51], s82 offen sc1\n"
"buffer_load_dwordx4 v[180:183], v10, s[48:51], s82 offen offset:1024 sc1\n"
"buffer_load_dwordx4 v[184:187], v10, s[48:51], s82 offen offset:2048 sc1\n"
"buffer_load_dwordx4 v[188:191], v10, s[48:51], s82 offen offset:3072 sc1\n"
"s_mov_b32 s82, 32768\n"
"buffer_load_dwordx4 v[192:195], v10, s[48:51], s82 offen sc1\n"
"buffer_load_dwordx4 v[196:199], v10, s[48:51], s82 offen offset:1024 sc1\n"
"s_waitcnt vmcnt(0)\n"
"v_max3_u32 v8, v136, v137, v138\n"
"v_max3_u32 v8, v139, v140, v8\n"
"v_max3_u32 v8, v141, v142, v8\n"
"v_max_u32 v8, v143, v8\n"
"v_cmp_eq_u32 vcc, -1, v8\n"
"s_sub_u32 s83, s83, 1\n"
"s_cbranch_scc1 .Lbk29\n"
"s_nop 2\n"
"s_cbranch_vccnz .Lsp109\n"
"s_branch .Lbk29\n"
".Lsl30:\n"
"s_mov_b32 s83, 50000\n"
".Lsp110:\n"
"s_sleep 4\n"
"s_mov_b32 s82, 4096\n"
"buffer_load_dwordx4 v[144:147], v10, s[48:51], s82 offen sc1\n"
"buffer_load_dwordx4 v[148:151], v10, s[48:51], s82 offen offset:1024 sc1\n"
"buffer_load_dwordx4 v[152:155], v10, s[48:51], s82 offen offset:2048 sc1\n"
"buffer_load_dwordx4 v[156:159], v10, s[48:51], s82 offen offset:3072 sc1\n"
"s_mov_b32 s82, 16384\n"
"buffer_load_dwordx4 v[160:163], v10, s[48:51], s82 offen sc1\n"
"buffer_load_dwordx4 v[164:167], v10, s[48:51], s82 offen offset:1024 sc1\n"
"buffer_load_dwordx4 v[168:171], v10, s[48:51], s82 offen offset:2048 sc1\n"
"buffer_load_dwordx4 v[172:175], v10, s[48:51], s82 offen offset:3072 sc1\n"
"s_mov_b32 s82, 20480\n"
"buffer_load_dwordx4 v[176:179], v10, s[48:51], s82 offen sc1\n"
"buffer_load_dwordx4 v[180:183], v10, s[48:51], s82 offen offset:1024 sc1\n"
"buffer_load_dwordx4 v[184:187], v10, s[48:51], s82 offen offset:2048 sc1\n"
"buffer_load_dwordx4 v[188:191], v10, s[48:51], s82 offen offset:3072 sc1\n"
"s_mov_b32 s82, 32768\n"
"buffer_load_dwordx4 v[192:195], v10, s[48:51], s82 offen sc1\n"
"buffer_load_dwordx4 v[196:199], v10, s[48:51], s82 offen offset:1024 sc1\n"
"buffer_load_dwordx4 v[200:203], v10, s[48:51], s82 offen offset:2048 sc1\n"
"buffer_load_dwordx4 v[204:207], v10, s[48:51], s82 offen offset:3072 sc1\n"
"s_waitcnt vmcnt(0)\n"
"v_max3_u32 v8, v144, v145, v146\n"
"v_max3_u32 v8, v147, v148, v8\n"
"v_max3_u32 v8, v149, v150, v8\n"
"v_max_u32 v8, v151, v8\n"
"v_cmp_eq_u32 vcc, -1, v8\n"
"s_sub_u32 s83, s83, 1\n"
"s_cbranch_scc1 .Lbk31\n"
"s_nop 2\n"
"s_cbranch_vccnz .Lsp110\n"
"s_branch .Lbk31\n"
".Lsl32:\n"
"s_mov_b32 s83, 50000\n"
".Lsp111:\n"
"s_sleep 4\n"
"s_mov_b32 s82, 4096\n"
"buffer_load_dwordx4 v[152:155], v10, s[48:51], s82 offen offset:2048 sc1\n"
"buffer_load_dwordx4 v[156:159], v10, s[48:51], s82 offen offset:3072 sc1\n"
"s_mov_b32 s82, 16384\n"
"buffer_load_dwordx4 v[160:163], v10, s[48:51], s82 offen sc1\n"
"buffer_load_dwordx4 v[164:167], v10, s[48:51], s82 offen offset:1024 sc1\n"
"buffer_load_dwordx4 v[168:171], v10, s[48:51], s82 offen offset:2048 sc1\n"
"buffer_load_dwordx4 v[172:175], v10, s[48:51], s82 offen offset:3072 sc1\n"
"s_mov_b32 s82, 20480\n"
"buffer_load_dwordx4 v[176:179], v10, s[48:51], s82 offen sc1\n"
"buffer_load_dwordx4 v[180:183], v10, s[48:51], s82 offen offset:1024 sc1\n"
"buffer_load_dwordx4 v[184:187], v10, s[48:51], s82 offen offset:2048 sc1\n"
"buffer_load_dwordx4 v[188:191], v10, s[48:51], s82 offen offset:3072 sc1\n"
"s_mov_b32 s82, 32768\n"
"buffer_load_dwordx4 v[192:195], v10, s[48:51], s82 offen sc1\n"
"buffer_load_dwordx4 v[196:199], v10, s[48:51], s82 offen offset:1024 sc1\n"
"buffer_load_dwordx4 v[200:203], v10, s[48:51], s82 offen offset:2048 sc1\n"
"buffer_load_dwordx4 v[204:207], v10, s[48:51], s82 offen offset:3072 sc1\n"
"s_mov_b32 s82, 36864\n"
"buffer_load_dwordx4 v[208:211], v10, s[48:51], s82 offen sc1\n"
"buffer_load_dwordx4 v[212:215], v10, s[48:51], s82 offen offset:1024 sc1\n"
"s_waitcnt vmcnt(0)\n"
"v_max3_u32 v8, v152, v153, v154\n"
"v_max3_u32 v8, v155, v156, v8\n"
"v_max3_u32 v8, v157, v158, v8\n"
"v_max_u32 v8, v159, v8\n"
"v_cmp_eq_u32 vcc, -1, v8\n"
"s_sub_u32 s83, s83, 1\n"
"s_cbranch_scc1 .Lbk33\n"
"s_nop 2\n"
"s_cbranch_vccnz .Lsp111\n"
"s_branch .Lbk33\n"
".Lsl34:\n"
"s_mov_b32 s83, 50000\n"
".Lsp112:\n"
"s_sleep 4\n"
"s_mov_b32 s82, 16384\n"
"buffer_load_dwordx4 v[160:163], v10, s[48:51], s82 offen sc1\n"
"buffer_load_dwordx4 v[164:167], v10, s[48:51], s82 offen offset:1024 sc1\n"
"buffer_load_dwordx4 v[168:171], v10, s[48:51], s82 offen offset:2048 sc1\n"
"buffer_load_dwordx4 v[172:175], v10, s[48:51], s82 offen offset:3072 sc1\n"
"s_mov_b32 s82, 20480\n"
"buffer_load_dwordx4 v[176:179], v10, s[48:51], s82 offen sc1\n"
"buffer_load_dwordx4 v[180:183], v10, s[48:51], s82 offen offset:1024 sc1\n"
"buffer_load_dwordx4 v[184:187], v10, s[48:51], s82 offen offset:2048 sc1\n"
"buffer_load_dwordx4 v[188:191], v10, s[48:51], s82 offen offset:3072 sc1\n"
"s_mov_b32 s82, 32768\n"
"buffer_load_dwordx4 v[192:195], v10, s[48:51], s82 offen sc1\n"
"buffer_load_dwordx4 v[196:199], v10, s[48:51], s82 offen offset:1024 sc1\n"
"buffer_load_dwordx4 v[200:203], v10, s[48:51], s82 offen offset:2048 sc1\n"
"buffer_load_dwordx4 v[204:207], v10, s[48:51], s82 offen offset:3072 sc1\n"
"s_mov_b32 s82, 36864\n"
"buffer_load_dwordx4 v[208:211], v10, s[48:51], s82 offen sc1\n"
"buffer_load_dwordx4 v[212:215], v10, s[48:51], s82 offen offset:1024 sc1\n"
"buffer_load_dwordx4 v[216:219], v10, s[48:51], s82 offen offset:2048 sc1\n"
"buffer_load_dwordx4 v[220:223], v10, s[48:51], s82 offen offset:3072 sc1\n"
"s_waitcnt vmcnt(0)\n"
"v_max3_u32 v8, v160, v161, v162\n"
"v_max3_u32 v8, v163, v164, v8\n"
"v_max3_u32 v8, v165, v166, v8\n"
"v_max_u32 v8, v167, v8\n"
"v_cmp_eq_u32 vcc, -1, v8\n"
"s_sub_u32 s83, s83, 1\n"
"s_cbranch_scc1 .Lbk35\n"
"s_nop 2\n"
"s_cbranch_vccnz .Lsp112\n"
"s_branch .Lbk35\n"
".Lsl36:\n"
"s_mov_b32 s83, 50000\n"
".Lsp113:\n"
"s_sleep 4\n"
"s_mov_b32 s82, 16384\n"
"buffer_load_dwordx4 v[168:171], v10, s[48:51], s82 offen offset:2048 sc1\n"
"buffer_load_dwordx4 v[172:175], v10, s[48:51], s82 offen offset:3072 sc1\n"
"s_mov_b32 s82, 20480\n"
"buffer_load_dwordx4 v[176:179], v10, s[48:51], s82 offen sc1\n"
"buffer_load_dwordx4 v[180:183], v10, s[48:51], s82 offen offset:1024 sc1\n"
"buffer_load_dwordx4 v[184:187], v10, s[48:51], s82 offen offset:2048 sc1\n"
"buffer_load_dwordx4 v[188:191], v10, s[48:51], s82 offen offset:3072 sc1\n"
"s_mov_b32 s82, 32768\n"
"buffer_load_dwordx4 v[192:195], v10, s[48:51], s82 offen sc1\n"
"buffer_load_dwordx4 v[196:199], v10, s[48:51], s82 offen offset:1024 sc1\n"
"buffer_load_dwordx4 v[200:203], v10, s[48:51], s82 offen offset:2048 sc1\n"
"buffer_load_dwordx4 v[204:207], v10, s[48:51], s82 offen offset:3072 sc1\n"
"s_mov_b32 s82, 36864\n"
"buffer_load_dwordx4 v[208:211], v10, s[48:51], s82 offen sc1\n"
"buffer_load_dwordx4 v[212:215], v10, s[48:51], s82 offen offset:1024 sc1\n"
"buffer_load_dwordx4 v[216:219], v10, s[48:51], s82 offen offset:2048 sc1\n"
"buffer_load_dwordx4 v[220:223], v10, s[48:51], s82 offen offset:3072 sc1\n"
"s_mov_b32 s82, 49152\n"
"buffer_load_dwordx4 a[224:227], v10, s[48:51], s82 offen sc1\n"
"buffer_load_dwordx4 a[228:231], v10, s[48:51], s82 offen offset:1024 sc1\n"
"s_waitcnt vmcnt(0)\n"
"v_max3_u32 v8, v168, v169, v170\n"
"v_max3_u32 v8, v171, v172, v8\n"
"v_max3_u32 v8, v173, v174, v8\n"
"v_max_u32 v8, v175, v8\n"
"v_cmp_eq_u32 vcc, -1, v8\n"
"s_sub_u32 s83, s83, 1\n"
"s_cbranch_scc1 .Lbk37\n"
"s_nop 2\n"
"s_cbranch_vccnz .Lsp113\n"
"s_branch .Lbk37\n"
".Lsl38:\n"
"s_mov_b32 s83, 50000\n"
".Lsp114:\n"
"s_sleep 4\n"
"s_mov_b32 s82, 20480\n"
"buffer_load_dwordx4 v[176:179], v10, s[48:51], s82 offen sc1\n"
"buffer_load_dwordx4 v[180:183], v10, s[48:51], s82 offen offset:1024 sc1\n"
"buffer_load_dwordx4 v[184:187], v10, s[48:51], s82 offen offset:2048 sc1\n"
"buffer_load_dwordx4 v[188:191], v10, s[48:51], s82 offen offset:3072 sc1\n"
"s_mov_b32 s82, 32768\n"
"buffer_load_dwordx4 v[192:195], v10, s[48:51], s82 offen sc1\n"
"buffer_load_dwordx4 v[196:199], v10, s[48:51], s82 offen offset:1024 sc1\n"
"buffer_load_dwordx4 v[200:203], v10, s[48:51], s82 offen offset:2048 sc1\n"
"buffer_load_dwordx4 v[204:207], v10, s[48:51], s82 offen offset:3072 sc1\n"
"s_mov_b32 s82, 36864\n"
"buffer_load_dwordx4 v[208:211], v10, s[48:51], s82 offen sc1\n"
"buffer_load_dwordx4 v[212:215], v10, s[48:51], s82 offen offset:1024 sc1\n"
"buffer_load_dwordx4 v[216:219], v10, s[48:51], s82 offen offset:2048 sc1\n"
"buffer_load_dwordx4 v[220:223], v10, s[48:51], s82 offen offset:3072 sc1\n"
"s_mov_b32 s82, 49152\n"
"buffer_load_dwordx4 a[224:227], v10, s[48:51], s82 offen sc1\n"
"buffer_load_dwordx4 a[228:231], v10, s[48:51], s82 offen offset:1024 sc1\n"
"buffer_load_dwordx4 a[232:235], v10, s[48:51], s82 offen offset:2048 sc1\n"
"buffer_load_dwordx4 a[236:239], v10, s[48:51], s82 offen offset:3072 sc1\n"
"s_waitcnt vmcnt(0)\n"
"v_max3_u32 v8, v176, v177, v178\n"
"v_max3_u32 v8, v179, v180, v8\n"
"v_max3_u32 v8, v181, v182, v8\n"
"v_max_u32 v8, v183, v8\n"
"v_cmp_eq_u32 vcc, -1, v8\n"
"s_sub_u32 s83, s83, 1\n"
"s_cbranch_scc1 .Lbk39\n"
"s_nop 2\n"
"s_cbranch_vccnz .Lsp114\n"
"s_branch .Lbk39\n"
".Lsl40:\n"
"s_mov_b32 s83, 50000\n"
".Lsp115:\n"
"s_sleep 4\n"
"s_mov_b32 s82, 20480\n"
"buffer_load_dwordx4 v[184:187], v10, s[48:51], s82 offen offset:2048 sc1\n"
"buffer_load_dwordx4 v[188:191], v10, s[48:51], s82 offen offset:3072 sc1\n"
"s_mov_b32 s82, 32768\n"
"buffer_load_dwordx4 v[192:195], v10, s[48:51], s82 offen sc1\n"
"buffer_load_dwordx4 v[196:199], v10, s[48:51], s82 offen offset:1024 sc1\n"
"buffer_load_dwordx4 v[200:203], v10, s[48:51], s82 offen offset:2048 sc1\n"
"buffer_load_dwordx4 v[204:207], v10, s[48:51], s82 offen offset:3072 sc1\n"
"s_mov_b32 s82, 36864\n"
"buffer_load_dwordx4 v[208:211], v10, s[48:51], s82 offen sc1\n"
"buffer_load_dwordx4 v[212:215], v10, s[48:51], s82 offen offset:1024 sc1\n"
"buffer_load_dwordx4 v[216:219], v10, s[48:51], s82 offen offset:2048 sc1\n"
"buffer_load_dwordx4 v[220:223], v10, s[48:51], s82 offen offset:3072 sc1\n"
"s_mov_b32 s82, 49152\n"
"buffer_load_dwordx4 a[224:227], v10, s[48:51], s82 offen sc1\n"
"buffer_load_dwordx4 a[228:231], v10, s[48:51], s82 offen offset:1024 sc1\n"
"buffer_load_dwordx4 a[232:235], v10, s[48:51], s82 offen offset:2048 sc1\n"
"buffer_load_dwordx4 a[236:239], v10, s[48:51], s82 offen offset:3072 sc1\n"
"s_mov_b32 s82, 53248\n"
"buffer_load_dwordx4 a[240:243], v10, s[48:51], s82 offen sc1\n"
"buffer_load_dwordx4 a[244:247], v10, s[48:51], s82 offen offset:1024 sc1\n"
"s_waitcnt vmcnt(0)\n"
"v_max3_u32 v8, v184, v185, v186\n"
"v_max3_u32 v8, v187, v188, v8\n"
"v_max3_u32 v8, v189, v190, v8\n"
"v_max_u32 v8, v191, v8\n"
"v_cmp_eq_u32 vcc, -1, v8\n"
"s_sub_u32 s83, s83, 1\n"
"s_cbranch_scc1 .Lbk41\n"
"s_nop 2\n"
"s_cbranch_vccnz .Lsp115\n"
"s_branch .Lbk41\n"
".Lsl42:\n"
"s_mov_b32 s83, 50000\n"
".Lsp116:\n"
"s_sleep 4\n"
"s_mov_b32 s82, 32768\n"
"buffer_load_dwordx4 v[192:195], v10, s[48:51], s82 offen sc1\n"
"buffer_load_dwordx4 v[196:199], v10, s[48:51], s82 offen offset:1024 sc1\n"
"buffer_load_dwordx4 v[200:203], v10, s[48:51], s82 offen offset:2048 sc1\n"
"buffer_load_dwordx4 v[204:207], v10, s[48:51], s82 offen offset:3072 sc1\n"
"s_mov_b32 s82, 36864\n"
"buffer_load_dwordx4 v[208:211], v10, s[48:51], s82 offen sc1\n"
"buffer_load_dwordx4 v[212:215], v10, s[48:51], s82 offen offset:1024 sc1\n"
"buffer_load_dwordx4 v[216:219], v10, s[48:51], s82 offen offset:2048 sc1\n"
"buffer_load_dwordx4 v[220:223], v10, s[48:51], s82 offen offset:3072 sc1\n"
"s_mov_b32 s82, 49152\n"
"buffer_load_dwordx4 a[224:227], v10, s[48:51], s82 offen sc1\n"
"buffer_load_dwordx4 a[228:231], v10, s[48:51], s82 offen offset:1024 sc1\n"
"buffer_load_dwordx4 a[232:235], v10, s[48:51], s82 offen offset:2048 sc1\n"
"buffer_load_dwordx4 a[236:239], v10, s[48:51], s82 offen offset:3072 sc1\n"
"s_mov_b32 s82, 53248\n"
"buffer_load_dwordx4 a[240:243], v10, s[48:51], s82 offen sc1\n"
"buffer_load_dwordx4 a[244:247], v10, s[48:51], s82 offen offset:1024 sc1\n"
"buffer_load_dwordx4 a[248:251], v10, s[48:51], s82 offen offset:2048 sc1\n"
"buffer_load_dwordx4 a[252:255], v10, s[48:51], s82 offen offset:3072 sc1\n"
"s_waitcnt vmcnt(0)\n"
"v_max3_u32 v8, v192, v193, v194\n"
"v_max3_u32 v8, v195, v196, v8\n"
"v_max3_u32 v8, v197, v198, v8\n"
"v_max_u32 v8, v199, v8\n"
"v_cmp_eq_u32 vcc, -1, v8\n"
"s_sub_u32 s83, s83, 1\n"
"s_cbranch_scc1 .Lbk43\n"
"s_nop 2\n"
"s_cbranch_vccnz .Lsp116\n"
"s_branch .Lbk43\n"
".Lsl44:\n"
"s_mov_b32 s83, 50000\n"
".Lsp117:\n"
"s_sleep 4\n"
"s_mov_b32 s82, 32768\n"
"buffer_load_dwordx4 v[200:203], v10, s[48:51], s82 offen offset:2048 sc1\n"
"buffer_load_dwordx4 v[204:207], v10, s[48:51], s82 offen offset:3072 sc1\n"
"s_mov_b32 s82, 36864\n"
"buffer_load_dwordx4 v[208:211], v10, s[48:51], s82 offen sc1\n"
"buffer_load_dwordx4 v[212:215], v10, s[48:51], s82 offen offset:1024 sc1\n"
"buffer_load_dwordx4 v[216:219], v10, s[48:51], s82 offen offset:2048 sc1\n"
"buffer_load_dwordx4 v[220:223], v10, s[48:51], s82 offen offset:3072 sc1\n"
"s_mov_b32 s82, 49152\n"
"buffer_load_dwordx4 a[224:227], v10, s[48:51], s82 offen sc1\n"
"buffer_load_dwordx4 a[228:231], v10, s[48:51], s82 offen offset:1024 sc1\n"
"buffer_load_dwordx4 a[232:235], v10, s[48:51], s82 offen offset:2048 sc1\n"
"buffer_load_dwordx4 a[236:239], v10, s[48:51], s82 offen offset:3072 sc1\n"
"s_mov_b32 s82, 53248\n"
"buffer_load_dwordx4 a[240:243], v10, s[48:51], s82 offen sc1\n"
"buffer_load_dwordx4 a[244:247], v10, s[48:51], s82 offen offset:1024 sc1\n"
"buffer_load_dwordx4 a[248:251], v10, s[48:51], s82 offen offset:2048 sc1\n"
"buffer_load_dwordx4 a[252:255], v10, s[48:51], s82 offen offset:3072 sc1\n"
"s_waitcnt vmcnt(0)\n"
"v_max3_u32 v8, v200, v201, v202\n"
"v_max3_u32 v8, v203, v204, v8\n"
"v_max3_u32 v8, v205, v206, v8\n"
"v_max_u32 v8, v207, v8\n"
"v_cmp_eq_u32 vcc, -1, v8\n"
"s_sub_u32 s83, s83, 1\n"
"s_cbranch_scc1 .Lbk45\n"
"s_nop 2\n"
"s_cbranch_vccnz .Lsp117\n"
"s_branch .Lbk45\n"
".Lsl46:\n"
"s_mov_b32 s83, 50000\n"
".Lsp118:\n"
"s_sleep 4\n"
"s_mov_b32 s82, 36864\n"
"buffer_load_dwordx4 v[208:211], v10, s[48:51], s82 offen sc1\n"
"buffer_load_dwordx4 v[212:215], v10, s[48:51], s82 offen offset:1024 sc1\n"
"buffer_load_dwordx4 v[216:219], v10, s[48:51], s82 offen offset:2048 sc1\n"
"buffer_load_dwordx4 v[220:223], v10, s[48:51], s82 offen offset:3072 sc1\n"
"s_mov_b32 s82, 49152\n"
"buffer_load_dwordx4 a[224:227], v10, s[48:51], s82 offen sc1\n"
"buffer_load_dwordx4 a[228:231], v10, s[48:51], s82 offen offset:1024 sc1\n"
"buffer_load_dwordx4 a[232:235], v10, s[48:51], s82 offen offset:2048 sc1\n"
"buffer_load_dwordx4 a[236:239], v10, s[48:51], s82 offen offset:3072 sc1\n"
"s_mov_b32 s82, 53248\n"
"buffer_load_dwordx4 a[240:243], v10, s[48:51], s82 offen sc1\n"
"buffer_load_dwordx4 a[244:247], v10, s[48:51], s82 offen offset:1024 sc1\n"
"buffer_load_dwordx4 a[248:251], v10, s[48:51], s82 offen offset:2048 sc1\n"
"buffer_load_dwordx4 a[252:255], v10, s[48:51], s82 offen offset:3072 sc1\n"
"s_waitcnt vmcnt(0)\n"
"v_max3_u32 v8, v208, v209, v210\n"
"v_max3_u32 v8, v211, v212, v8\n"
"v_max3_u32 v8, v213, v214, v8\n"
"v_max_u32 v8, v215, v8\n"
"v_cmp_eq_u32 vcc, -1, v8\n"
"s_sub_u32 s83, s83, 1\n"
"s_cbranch_scc1 .Lbk47\n"
"s_nop 2\n"
"s_cbranch_vccnz .Lsp118\n"
"s_branch .Lbk47\n"
".Lsl48:\n"
"s_mov_b32 s83, 50000\n"
".Lsp119:\n"
"s_sleep 4\n"
"s_mov_b32 s82, 36864\n"
"buffer_load_dwordx4 v[216:219], v10, s[48:51], s82 offen offset:2048 sc1\n"
"buffer_load_dwordx4 v[220:223], v10, s[48:51], s82 offen offset:3072 sc1\n"
"s_mov_b32 s82, 49152\n"
"buffer_load_dwordx4 a[224:227], v10, s[48:51], s82 offen sc1\n"
"buffer_load_dwordx4 a[228:231], v10, s[48:51], s82 offen offset:1024 sc1\n"
"buffer_load_dwordx4 a[232:235], v10, s[48:51], s82 offen offset:2048 sc1\n"
"buffer_load_dwordx4 a[236:239], v10, s[48:51], s82 offen offset:3072 sc1\n"
"s_mov_b32 s82, 53248\n"
"buffer_load_dwordx4 a[240:243], v10, s[48:51], s82 offen sc1\n"
"buffer_load_dwordx4 a[244:247], v10, s[48:51], s82 offen offset:1024 sc1\n"
"buffer_load_dwordx4 a[248:251], v10, s[48:51], s82 offen offset:2048 sc1\n"
"buffer_load_dwordx4 a[252:255], v10, s[48:51], s82 offen offset:3072 sc1\n"
"s_waitcnt vmcnt(0)\n"
"v_max3_u32 v8, v216, v217, v218\n"
"v_max3_u32 v8, v219, v220, v8\n"
"v_max3_u32 v8, v221, v222, v8\n"
"v_max_u32 v8, v223, v8\n"
"v_cmp_eq_u32 vcc, -1, v8\n"
"s_sub_u32 s83, s83, 1\n"
"s_cbranch_scc1 .Lbk49\n"
"s_nop 2\n"
"s_cbranch_vccnz .Lsp119\n"
"s_branch .Lbk49\n"
".Lsl50:\n"
"s_mov_b32 s83, 50000\n"
".Lsp120:\n"
"s_sleep 4\n"
"s_mov_b32 s82, 49152\n"
"buffer_load_dwordx4 a[224:227], v10, s[48:51], s82 offen sc1\n"
"buffer_load_dwordx4 a[228:231], v10, s[48:51], s82 offen offset:1024 sc1\n"
"buffer_load_dwordx4 a[232:235], v10, s[48:51], s82 offen offset:2048 sc1\n"
"buffer_load_dwordx4 a[236:239], v10, s[48:51], s82 offen offset:3072 sc1\n"
"s_mov_b32 s82, 53248\n"
"buffer_load_dwordx4 a[240:243], v10, s[48:51], s82 offen sc1\n"
"buffer_load_dwordx4 a[244:247], v10, s[48:51], s82 offen offset:1024 sc1\n"
"buffer_load_dwordx4 a[248:251], v10, s[48:51], s82 offen offset:2048 sc1\n"
"buffer_load_dwordx4 a[252:255], v10, s[48:51], s82 offen offset:3072 sc1\n"
"s_waitcnt vmcnt(0)\n"
"v_accvgpr_read_b32 v18, a224\n"
"v_accvgpr_read_b32 v19, a225\n"
"v_max_u32 v8, v18, v19\n"
"v_accvgpr_read_b32 v18, a226\n"
"v_accvgpr_read_b32 v19, a227\n"
"v_max3_u32 v8, v18, v19, v8\n"
"v_accvgpr_read_b32 v18, a228\n"
"v_accvgpr_read_b32 v19, a229\n"
"v_max3_u32 v8, v18, v19, v8\n"
"v_accvgpr_read_b32 v18, a230\n"
"v_accvgpr_read_b32 v19, a231\n"
"v_max3_u32 v8, v18, v19, v8\n"
"v_cmp_eq_u32 vcc, -1, v8\n"
"s_sub_u32 s83, s83, 1\n"
"s_cbranch_scc1 .Lbk51\n"
"s_nop 2\n"
"s_cbranch_vccnz .Lsp120\n"
"s_branch .Lbk51\n"
".Lsl52:\n"
"s_mov_b32 s83, 50000\n"
".Lsp121:\n"
"s_sleep 4\n"
"s_mov_b32 s82, 49152\n"
"buffer_load_dwordx4 a[232:235], v10, s[48:51], s82 offen offset:2048 sc1\n"
"buffer_load_dwordx4 a[236:239], v10, s[48:51], s82 offen offset:3072 sc1\n"
"s_mov_b32 s82, 53248\n"
"buffer_load_dwordx4 a[240:243], v10, s[48:51], s82 offen sc1\n"
"buffer_load_dwordx4 a[244:247], v10, s[48:51], s82 offen offset:1024 sc1\n"
"buffer_load_dwordx4 a[248:251], v10, s[48:51], s82 offen offset:2048 sc1\n"
"buffer_load_dwordx4 a[252:255], v10, s[48:51], s82 offen offset:3072 sc1\n"
"s_waitcnt vmcnt(0)\n"
"v_accvgpr_read_b32 v18, a232\n"
"v_accvgpr_read_b32 v19, a233\n"
"v_max_u32 v8, v18, v19\n"
"v_accvgpr_read_b32 v18, a234\n"
"v_accvgpr_read_b32 v19, a235\n"
"v_max3_u32 v8, v18, v19, v8\n"
"v_accvgpr_read_b32 v18, a236\n"
"v_accvgpr_read_b32 v19, a237\n"
"v_max3_u32 v8, v18, v19, v8\n"
"v_accvgpr_read_b32 v18, a238\n"
"v_accvgpr_read_b32 v19, a239\n"
"v_max3_u32 v8, v18, v19, v8\n"
"v_cmp_eq_u32 vcc, -1, v8\n"
"s_sub_u32 s83, s83, 1\n"
"s_cbranch_scc1 .Lbk53\n"
"s_nop 2\n"
"s_cbranch_vccnz .Lsp121\n"
"s_branch .Lbk53\n"
".Lsl54:\n"
"s_mov_b32 s83, 50000\n"
".Lsp122:\n"
"s_sleep 4\n"
"s_mov_b32 s82, 53248\n"
"buffer_load_dwordx4 a[240:243], v10, s[48:51], s82 offen sc1\n"
"buffer_load_dwordx4 a[244:247], v10, s[48:51], s82 offen offset:1024 sc1\n"
"buffer_load_dwordx4 a[248:251], v10, s[48:51], s82 offen offset:2048 sc1\n"
"buffer_load_dwordx4 a[252:255], v10, s[48:51], s82 offen offset:3072 sc1\n"
"s_waitcnt vmcnt(0)\n"
"v_accvgpr_read_b32 v18, a240\n"
"v_accvgpr_read_b32 v19, a241\n"
"v_max_u32 v8, v18, v19\n"
"v_accvgpr_read_b32 v18, a242\n"
"v_accvgpr_read_b32 v19, a243\n"
"v_max3_u32 v8, v18, v19, v8\n"
"v_accvgpr_read_b32 v18, a244\n"
"v_accvgpr_read_b32 v19, a245\n"
"v_max3_u32 v8, v18, v19, v8\n"
"v_accvgpr_read_b32 v18, a246\n"
"v_accvgpr_read_b32 v19, a247\n"
"v_max3_u32 v8, v18, v19, v8\n"
"v_cmp_eq_u32 vcc, -1, v8\n"
"s_sub_u32 s83, s83, 1\n"
"s_cbranch_scc1 .Lbk55\n"
"s_nop 2\n"
"s_cbranch_vccnz .Lsp122\n"
"s_branch .Lbk55\n"
".Lsl56:\n"
"s_mov_b32 s83, 50000\n"
".Lsp123:\n"
"s_sleep 4\n"
"s_mov_b32 s82, 53248\n"
"buffer_load_dwordx4 a[248:251], v10, s[48:51], s82 offen offset:2048 sc1\n"
"buffer_load_dwordx4 a[252:255], v10, s[48:51], s82 offen offset:3072 sc1\n"
"s_waitcnt vmcnt(0)\n"
"v_accvgpr_read_b32 v18, a248\n"
"v_accvgpr_read_b32 v19, a249\n"
"v_max_u32 v8, v18, v19\n"
"v_accvgpr_read_b32 v18, a250\n"
"v_accvgpr_read_b32 v19, a251\n"
"v_max3_u32 v8, v18, v19, v8\n"
"v_accvgpr_read_b32 v18, a252\n"
"v_accvgpr_read_b32 v19, a253\n"
"v_max3_u32 v8, v18, v19, v8\n"
"v_accvgpr_read_b32 v18, a254\n"
"v_accvgpr_read_b32 v19, a255\n"
"v_max3_u32 v8, v18, v19, v8\n"
"v_cmp_eq_u32 vcc, -1, v8\n"
"s_sub_u32 s83, s83, 1\n"
"s_cbranch_scc1 .Lbk57\n"
"s_nop 2\n"
"s_cbranch_vccnz .Lsp123\n"
"s_branch .Lbk57\n"
".Lsl58:\n"
"s_mov_b32 s83, 50000\n"
".Lsp124:\n"
"s_sleep 4\n"
"buffer_load_dwordx4 v[40:43], v1, s[48:51], s88 offen sc1\n"
"buffer_load_dwordx4 v[44:47], v1, s[48:51], s88 offen offset:1024 sc1\n"
"buffer_load_dwordx4 v[48:51], v1, s[48:51], s88 offen offset:2048 sc1\n"
"buffer_load_dwordx4 v[52:55], v1, s[48:51], s88 offen offset:3072 sc1\n"
"s_waitcnt vmcnt(0)\n"
"v_max3_u32 v9, v40, v41, v42\n"
"v_max3_u32 v9, v43, v44, v9\n"
"v_max3_u32 v9, v45, v46, v9\n"
"v_max3_u32 v9, v47, v48, v9\n"
"v_max3_u32 v9, v49, v50, v9\n"
"v_max3_u32 v9, v51, v52, v9\n"
"v_max3_u32 v9, v53, v54, v9\n"
"v_max_u32 v9, v55, v9\n"
"v_cmp_eq_u32 s[86:87], -1, v9\n"
"s_sub_u32 s83, s83, 1\n"
"s_cbranch_scc1 .Lbk59\n"
"s_cmp_lg_u64 s[86:87], 0\n"
"s_cbranch_scc1 .Lsp124\n"
"s_branch .Lbk59\n"
".Lsl63:\n"
"s_mov_b32 s83, 50000\n"
".Lsp125:\n"
"s_sleep 4\n"
"buffer_load_dwordx4 v[160:163], v1, s[48:51], s20 offen offset:2048 sc1\n"
"buffer_load_dwordx4 v[164:167], v1, s[48:51], s21 offen offset:2048 sc1\n"
"buffer_load_dwordx4 v[168:171], v1, s[48:51], s22 offen offset:2048 sc1\n"
"buffer_load_dwordx4 v[172:175], v1, s[48:51], s23 offen offset:2048 sc1\n"
"buffer_load_dwordx4 v[176:179], v1, s[48:51], s24 offen offset:2048 sc1\n"
"buffer_load_dwordx4 v[180:183], v1, s[48:51], s25 offen offset:2048 sc1\n"
"buffer_load_dwordx4 v[184:187], v1, s[48:51], s26 offen offset:2048 sc1\n"
"buffer_load_dwordx4 v[188:191], v1, s[48:51], s27 offen offset:2048 sc1\n"
"buffer_load_dwordx4 v[192:195], v1, s[48:51], s20 offen offset:3072 sc1\n"
"buffer_load_dwordx4 v[196:199], v1, s[48:51], s21 offen offset:3072 sc1\n"
"buffer_load_dwordx4 v[200:203], v1, s[48:51], s22 offen offset:3072 sc1\n"
"buffer_load_dwordx4 v[204:207], v1, s[48:51], s23 offen offset:3072 sc1\n"
"buffer_load_dwordx4 v[208:211], v1, s[48:51], s24 offen offset:3072 sc1\n"
"buffer_load_dwordx4 v[212:215], v1, s[48:51], s25 offen offset:3072 sc1\n"
"s_waitcnt vmcnt(0)\n"
"v_max3_u32 v8, v160, v161, v162\n"
"v_max3_u32 v8, v163, v164, v8\n"
"v_max3_u32 v8, v165, v166, v8\n"
"v_max_u32 v8, v167, v8\n"
"v_cmp_eq_u32 vcc, -1, v8\n"
"s_sub_u32 s83, s83, 1\n"
"s_cbranch_scc1 .Lbk64\n"
"s_nop 2\n"
"s_cbranch_vccnz .Lsp125\n"
"s_branch .Lbk64\n"
".Lsl65:\n"
"s_mov_b32 s83, 50000\n"
".Lsp126:\n"
"s_sleep 4\n"
"buffer_load_dwordx4 v[168:171], v1, s[48:51], s22 offen offset:2048 sc1\n"
"buffer_load_dwordx4 v[172:175], v1, s[48:51], s23 offen offset:2048 sc1\n"
"buffer_load_dwordx4 v[176:179], v1, s[48:51], s24 offen offset:2048 sc1\n"
"buffer_load_dwordx4 v[180:183], v1, s[48:51], s25 offen offset:2048 sc1\n"
"buffer_load_dwordx4 v[184:187], v1, s[48:51], s26 offen offset:2048 sc1\n"
"buffer_load_dwordx4 v[188:191], v1, s[48:51], s27 offen offset:2048 sc1\n"
"buffer_load_dwordx4 v[192:195], v1, s[48:51], s20 offen offset:3072 sc1\n"
"buffer_load_dwordx4 v[196:199], v1, s[48:51], s21 offen offset:3072 sc1\n"
"buffer_load_dwordx4 v[200:203], v1, s[48:51], s22 offen offset:3072 sc1\n"
"buffer_load_dwordx4 v[204:207], v1, s[48:51], s23 offen offset:3072 sc1\n"
"buffer_load_dwordx4 v[208:211], v1, s[48:51], s24 offen offset:3072 sc1\n"
"buffer_load_dwordx4 v[212:215], v1, s[48:51], s25 offen offset:3072 sc1\n"
"buffer_load_dwordx4 v[216:219], v1, s[48:51], s26 offen offset:3072 sc1\n"
"buffer_load_dwordx4 v[220:223], v1, s[48:51], s27 offen offset:3072 sc1\n"
"s_waitcnt vmcnt(0)\n"
"v_max3_u32 v8, v168, v169, v170\n"
"v_max3_u32 v8, v171, v172, v8\n"
"v_max3_u32 v8, v173, v174, v8\n"
"v_max_u32 v8, v175, v8\n"
"v_cmp_eq_u32 vcc, -1, v8\n"
"s_sub_u32 s83, s83, 1\n"
"s_cbranch_scc1 .Lbk66\n"
"s_nop 2\n"
"s_cbranch_vccnz .Lsp126\n"
"s_branch .Lbk66\n"
".Lsl67:\n"
"s_mov_b32 s83, 50000\n"
".Lsp127:\n"
"s_sleep 4\n"
"buffer_load_dwordx4 v[176:179], v1, s[48:51], s24 offen offset:2048 sc1\n"
"buffer_load_dwordx4 v[180:183], v1, s[48:51], s25 offen offset:2048 sc1\n"
"buffer_load_dwordx4 v[184:187], v1, s[48:51], s26 offen offset:2048 sc1\n"
"buffer_load_dwordx4 v[188:191], v1, s[48:51], s27 offen offset:2048 sc1\n"
"buffer_load_dwordx4 v[192:195], v1, s[48:51], s20 offen offset:3072 sc1\n"
"buffer_load_dwordx4 v[196:199], v1, s[48:51], s21 offen offset:3072 sc1\n"
"buffer_load_dwordx4 v[200:203], v1, s[48:51], s22 offen offset:3072 sc1\n"
"buffer_load_dwordx4 v[204:207], v1, s[48:51], s23 offen offset:3072 sc1\n"
"buffer_load_dwordx4 v[208:211], v1, s[48:51], s24 offen offset:3072 sc1\n"
"buffer_load_dwordx4 v[212:215], v1, s[48:51], s25 offen offset:3072 sc1\n"
"buffer_load_dwordx4 v[216:219], v1, s[48:51], s26 offen offset:3072 sc1\n"
"buffer_load_dwordx4 v[220:223], v1, s[48:51], s27 offen offset:3072 sc1\n"
"buffer_load_dwordx4 v[96:99], v30, s[48:51], s20 offen sc1\n"
"buffer_load_dwordx4 v[100:103], v30, s[48:51], s21 offen sc1\n"
"s_waitcnt vmcnt(0)\n"
"v_max3_u32 v8, v176, v177, v178\n"
"v_max3_u32 v8, v179, v180, v8\n"
"v_max3_u32 v8, v181, v182, v8\n"
"v_max_u32 v8, v183, v8\n"
"v_cmp_eq_u32 vcc, -1, v8\n"
"s_sub_u32 s83, s83, 1\n"
"s_cbranch_scc1 .Lbk68\n"
"s_nop 2\n"
"s_cbranch_vccnz .Lsp127\n"
"s_branch .Lbk68\n"
".Lsl69:\n"
"s_mov_b32 s83, 50000\n"
".Lsp128:\n"
"s_sleep 4\n"
"buffer_load_dwordx4 v[184:187], v1, s[48:51], s26 offen offset:2048 sc1\n"
"buffer_load_dwordx4 v[188:191], v1, s[48:51], s27 offen offset:2048 sc1\n"
"buffer_load_dwordx4 v[192:195], v1, s[48:51], s20 offen offset:3072 sc1\n"
"buffer_load_dwordx4 v[196:199], v1, s[48:51], s21 offen offset:3072 sc1\n"
"buffer_load_dwordx4 v[200:203], v1, s[48:51], s22 offen offset:3072 sc1\n"
"buffer_load_dwordx4 v[204:207], v1, s[48:51], s23 offen offset:3072 sc1\n"
"buffer_load_dwordx4 v[208:211], v1, s[48:51], s24 offen offset:3072 sc1\n"
"buffer_load_dwordx4 v[212:215], v1, s[48:51], s25 offen offset:3072 sc1\n"
"buffer_load_dwordx4 v[216:219], v1, s[48:51], s26 offen offset:3072 sc1\n"
"buffer_load_dwordx4 v[220:223], v1, s[48:51], s27 offen offset:3072 sc1\n"
"buffer_load_dwordx4 v[96:99], v30, s[48:51], s20 offen sc1\n"
"buffer_load_dwordx4 v[100:103], v30, s[48:51], s21 offen sc1\n"
"buffer_load_dwordx4 v[104:107], v30, s[48:51], s22 offen sc1\n"
"buffer_load_dwordx4 v[108:111], v30, s[48:51], s23 offen sc1\n"
"s_waitcnt vmcnt(0)\n"
"v_max3_u32 v8, v184, v185, v186\n"
"v_max3_u32 v8, v187, v188, v8\n"
"v_max3_u32 v8, v189, v190, v8\n"
"v_max_u32 v8, v191, v8\n"
"v_cmp_eq_u32 vcc, -1, v8\n"
"s_sub_u32 s83, s83, 1\n"
"s_cbranch_scc1 .Lbk70\n"
"s_nop 2\n"
"s_cbranch_vccnz .Lsp128\n"
"s_branch .Lbk70\n"
".Lsl71:\n"
"s_mov_b32 s83, 50000\n"
".Lsp129:\n"
"s_sleep 4\n"
"buffer_load_dwordx4 v[192:195], v1, s[48:51], s20 offen offset:3072 sc1\n"
"buffer_load_dwordx4 v[196:199], v1, s[48:51], s21 offen offset:3072 sc1\n"
"buffer_load_dwordx4 v[200:203], v1, s[48:51], s22 offen offset:3072 sc1\n"
"buffer_load_dwordx4 v[204:207], v1, s[48:51], s23 offen offset:3072 sc1\n"
"buffer_load_dwordx4 v[208:211], v1, s[48:51], s24 offen offset:3072 sc1\n"
"buffer_load_dwordx4 v[212:215], v1, s[48:51], s25 offen offset:3072 sc1\n"
"buffer_load_dwordx4 v[216:219], v1, s[48:51], s26 offen offset:3072 sc1\n"
"buffer_load_dwordx4 v[220:223], v1, s[48:51], s27 offen offset:3072 sc1\n"
"buffer_load_dwordx4 v[96:99], v30, s[48:51], s20 offen sc1\n"
"buffer_load_dwordx4 v[100:103], v30, s[48:51], s21 offen sc1\n"
"buffer_load_dwordx4 v[104:107], v30, s[48:51], s22 offen sc1\n"
"buffer_load_dwordx4 v[108:111], v30, s[48:51], s23 offen sc1\n"
"buffer_load_dwordx4 v[56:59], v30, s[48:51], s24 offen sc1\n"
"buffer_load_dwordx4 v[60:63], v30, s[48:51], s25 offen sc1\n"
"s_waitcnt vmcnt(0)\n"
"v_max3_u32 v8, v192, v193, v194\n"
"v_max3_u32 v8, v195, v196, v8\n"
"v_max3_u32 v8, v197, v198, v8\n"
"v_max_u32 v8, v199, v8\n"
"v_cmp_eq_u32 vcc, -1, v8\n"
"s_sub_u32 s83, s83, 1\n"
"s_cbranch_scc1 .Lbk72\n"
"s_nop 2\n"
"s_cbranch_vccnz .Lsp129\n"
"s_branch .Lbk72\n"
".Lsl73:\n"
"s_mov_b32 s83, 50000\n"
".Lsp130:\n"
"s_sleep 4\n"
"buffer_load_dwordx4 v[200:203], v1, s[48:51], s22 offen offset:3072 sc1\n"
"buffer_load_dwordx4 v[204:207], v1, s[48:51], s23 offen offset:3072 sc1\n"
"buffer_load_dwordx4 v[208:211], v1, s[48:51], s24 offen offset:3072 sc1\n"
"buffer_load_dwordx4 v[212:215], v1, s[48:51], s25 offen offset:3072 sc1\n"
"buffer_load_dwordx4 v[216:219], v1, s[48:51], s26 offen offset:3072 sc1\n"
"buffer_load_dwordx4 v[220:223], v1, s[48:51], s27 offen offset:3072 sc1\n"
"buffer_load_dwordx4 v[96:99], v30, s[48:51], s20 offen sc1\n"
"buffer_load_dwordx4 v[100:103], v30, s[48:51], s21 offen sc1\n"
"buffer_load_dwordx4 v[104:107], v30, s[48:51], s22 offen sc1\n"
"buffer_load_dwordx4 v[108:111], v30, s[48:51], s23 offen sc1\n"
"buffer_load_dwordx4 v[56:59], v30, s[48:51], s24 offen sc1\n"
"buffer_load_dwordx4 v[60:63], v30, s[48:51], s25 offen sc1\n"
"buffer_load_dwordx4 v[224:227], v30, s[48:51], s26 offen sc1\n"
"buffer_load_dwordx4 v[228:231], v30, s[48:51], s27 offen sc1\n"
"s_waitcnt vmcnt(0)\n"
"v_max3_u32 v8, v200, v201, v202\n"
"v_max3_u32 v8, v203, v204, v8\n"
"v_max3_u32 v8, v205, v206, v8\n"
"v_max_u32 v8, v207, v8\n"
"v_cmp_eq_u32 vcc, -1, v8\n"
"s_sub_u32 s83, s83, 1\n"
"s_cbranch_scc1 .Lbk74\n"
"s_nop 2\n"
"s_cbranch_vccnz .Lsp130\n"
"s_branch .Lbk74\n"
".Lsl75:\n"
"s_mov_b32 s83, 50000\n"
".Lsp131:\n"
"s_sleep 4\n"
"buffer_load_dwordx4 v[208:211], v1, s[48:51], s24 offen offset:3072 sc1\n"
"buffer_load_dwordx4 v[212:215], v1, s[48:51], s25 offen offset:3072 sc1\n"
"buffer_load_dwordx4 v[216:219], v1, s[48:51], s26 offen offset:3072 sc1\n"
"buffer_load_dwordx4 v[220:223], v1, s[48:51], s27 offen offset:3072 sc1\n"
"buffer_load_dwordx4 v[96:99], v30, s[48:51], s20 offen sc1\n"
"buffer_load_dwordx4 v[100:103], v30, s[48:51], s21 offen sc1\n"
"buffer_load_dwordx4 v[104:107], v30, s[48:51], s22 offen sc1\n"
"buffer_load_dwordx4 v[108:111], v30, s[48:51], s23 offen sc1\n"
"buffer_load_dwordx4 v[56:59], v30, s[48:51], s24 offen sc1\n"
"buffer_load_dwordx4 v[60:63], v30, s[48:51], s25 offen sc1\n"
"buffer_load_dwordx4 v[224:227], v30, s[48:51], s26 offen sc1\n"
"buffer_load_dwordx4 v[228:231], v30, s[48:51], s27 offen sc1\n"
"buffer_load_dwordx4 v[232:235], v30, s[48:51], s20 offen offset:1024 sc1\n"
"buffer_load_dwordx4 v[236:239], v30, s[48:51], s21 offen offset:1024 sc1\n"
"s_waitcnt vmcnt(0)\n"
"v_max3_u32 v8, v208, v209, v210\n"
"v_max3_u32 v8, v211, v212, v8\n"
"v_max3_u32 v8, v213, v214, v8\n"
"v_max_u32 v8, v215, v8\n"
"v_cmp_eq_u32 vcc, -1, v8\n"
"s_sub_u32 s83, s83, 1\n"
"s_cbranch_scc1 .Lbk76\n"
"s_nop 2\n"
"s_cbranch_vccnz .Lsp131\n"
"s_branch .Lbk76\n"
".Lsl77:\n"
"s_mov_b32 s83, 50000\n"
".Lsp132:\n"
"s_sleep 4\n"
"buffer_load_dwordx4 v[216:219], v1, s[48:51], s26 offen offset:3072 sc1\n"
"buffer_load_dwordx4 v[220:223], v1, s[48:51], s27 offen offset:3072 sc1\n"
"buffer_load_dwordx4 v[96:99], v30, s[48:51], s20 offen sc1\n"
"buffer_load_dwordx4 v[100:103], v30, s[48:51], s21 offen sc1\n"
"buffer_load_dwordx4 v[104:107], v30, s[48:51], s22 offen sc1\n"
"buffer_load_dwordx4 v[108:111], v30, s[48:51], s23 offen sc1\n"
"buffer_load_dwordx4 v[56:59], v30, s[48:51], s24 offen sc1\n"
"buffer_load_dwordx4 v[60:63], v30, s[48:51], s25 offen sc1\n"
"buffer_load_dwordx4 v[224:227], v30, s[48:51], s26 offen sc1\n"
"buffer_load_dwordx4 v[228:231], v30, s[48:51], s27 offen sc1\n"
"buffer_load_dwordx4 v[232:235], v30, s[48:51], s20 offen offset:1024 sc1\n"
"buffer_load_dwordx4 v[236:239], v30, s[48:51], s21 offen offset:1024 sc1\n"
"buffer_load_dwordx4 v[128:131], v30, s[48:51], s22 offen offset:1024 sc1\n"
"buffer_load_dwordx4 v[132:135], v30, s[48:51], s23 offen offset:1024 sc1\n"
"s_waitcnt vmcnt(0)\n"
"v_max3_u32 v8, v216, v217, v218\n"
"v_max3_u32 v8, v219, v220, v8\n"
"v_max3_u32 v8, v221, v222, v8\n"
"v_max_u32 v8, v223, v8\n"
"v_cmp_eq_u32 vcc, -1, v8\n"
"s_sub_u32 s83, s83, 1\n"
"s_cbranch_scc1 .Lbk78\n"
"s_nop 2\n"
"s_cbranch_vccnz .Lsp132\n"
"s_branch .Lbk78\n"
".Lsl79:\n"
"s_mov_b32 s83, 50000\n"
".Lsp133:\n"
"s_sleep 4\n"
"buffer_load_dwordx4 v[96:99], v30, s[48:51], s20 offen sc1\n"
"buffer_load_dwordx4 v[100:103], v30, s[48:51], s21 offen sc1\n"
"buffer_load_dwordx4 v[104:107], v30, s[48:51], s22 offen sc1\n"
"buffer_load_dwordx4 v[108:111], v30, s[48:51], s23 offen sc1\n"
"buffer_load_dwordx4 v[56:59], v30, s[48:51], s24 offen sc1\n"
"buffer_load_dwordx4 v[60:63], v30, s[48:51], s25 offen sc1\n"
"buffer_load_dwordx4 v[224:227], v30, s[48:51], s26 offen sc1\n"
"buffer_load_dwordx4 v[228:231], v30, s[48:51], s27 offen sc1\n"
"buffer_load_dwordx4 v[232:235], v30, s[48:51], s20 offen offset:1024 sc1\n"
"buffer_load_dwordx4 v[236:239], v30, s[48:51], s21 offen offset:1024 sc1\n"
"buffer_load_dwordx4 v[128:131], v30, s[48:51], s22 offen offset:1024 sc1\n"
"buffer_load_dwordx4 v[132:135], v30, s[48:51], s23 offen offset:1024 sc1\n"
"buffer_load_dwordx4 v[136:139], v30, s[48:51], s24 offen offset:1024 sc1\n"
"buffer_load_dwordx4 v[80:83], v30, s[48:51], s25 offen offset:1024 sc1\n"
"s_waitcnt vmcnt(0)\n"
"v_max3_u32 v8, v96, v97, v98\n"
"v_max3_u32 v8, v99, v100, v8\n"
"v_max3_u32 v8, v101, v102, v8\n"
"v_max_u32 v8, v103, v8\n"
"v_cmp_eq_u32 vcc, -1, v8\n"
"s_sub_u32 s83, s83, 1\n"
"s_cbranch_scc1 .Lbk80\n"
"s_nop 2\n"
"s_cbranch_vccnz .Lsp133\n"
"s_branch .Lbk80\n"
".Lsl81:\n"
"s_mov_b32 s83, 50000\n"
".Lsp134:\n"
"s_sleep 4\n"
"buffer_load_dwordx4 v[104:107], v30, s[48:51], s22 offen sc1\n"
"buffer_load_dwordx4 v[108:111], v30, s[48:51], s23 offen sc1\n"
"buffer_load_dwordx4 v[56:59], v30, s[48:51], s24 offen sc1\n"
"buffer_load_dwordx4 v[60:63], v30, s[48:51], s25 offen sc1\n"
"buffer_load_dwordx4 v[224:227], v30, s[48:51], s26 offen sc1\n"
"buffer_load_dwordx4 v[228:231], v30, s[48:51], s27 offen sc1\n"
"buffer_load_dwordx4 v[232:235], v30, s[48:51], s20 offen offset:1024 sc1\n"
"buffer_load_dwordx4 v[236:239], v30, s[48:51], s21 offen offset:1024 sc1\n"
"buffer_load_dwordx4 v[128:131], v30, s[48:51], s22 offen offset:1024 sc1\n"
"buffer_load_dwordx4 v[132:135], v30, s[48:51], s23 offen offset:1024 sc1\n"
"buffer_load_dwordx4 v[136:139], v30, s[48:51], s24 offen offset:1024 sc1\n"
"buffer_load_dwordx4 v[80:83], v30, s[48:51], s25 offen offset:1024 sc1\n"
"buffer_load_dwordx4 v[84:87], v30, s[48:51], s26 offen offset:1024 sc1\n"
"buffer_load_dwordx4 v[88:91], v30, s[48:51], s27 offen offset:1024 sc1\n"
"s_waitcnt vmcnt(0)\n"
"v_max3_u32 v8, v104, v105, v106\n"
"v_max3_u32 v8, v107, v108, v8\n"
"v_max3_u32 v8, v109, v110, v8\n"
"v_max_u32 v8, v111, v8\n"
"v_cmp_eq_u32 vcc, -1, v8\n"
"s_sub_u32 s83, s83, 1\n"
"s_cbranch_scc1 .Lbk82\n"
"s_nop 2\n"
"s_cbranch_vccnz .Lsp134\n"
"s_branch .Lbk82\n"
".Lsl83:\n"
"s_mov_b32 s83, 50000\n"
".Lsp135:\n"
"s_sleep 4\n"
"buffer_load_dwordx4 v[56:59], v30, s[48:51], s24 offen sc1\n"
"buffer_load_dwordx4 v[60:63], v30, s[48:51], s25 offen sc1\n"
"buffer_load_dwordx4 v[224:227], v30, s[48:51], s26 offen sc1\n"
"buffer_load_dwordx4 v[228:231], v30, s[48:51], s27 offen sc1\n"
"buffer_load_dwordx4 v[232:235], v30, s[48:51], s20 offen offset:1024 sc1\n"
"buffer_load_dwordx4 v[236:239], v30, s[48:51], s21 offen offset:1024 sc1\n"
"buffer_load_dwordx4 v[128:131], v30, s[48:51], s22 offen offset:1024 sc1\n"
"buffer_load_dwordx4 v[132:135], v30, s[48:51], s23 offen offset:1024 sc1\n"
"buffer_load_dwordx4 v[136:139], v30, s[48:51], s24 offen offset:1024 sc1\n"
"buffer_load_dwordx4 v[80:83], v30, s[48:51], s25 offen offset:1024 sc1\n"
"buffer_load_dwordx4 v[84:87], v30, s[48:51], s26 offen offset:1024 sc1\n"
"buffer_load_dwordx4 v[88:91], v30, s[48:51], s27 offen offset:1024 sc1\n"
"buffer_load_dwordx4 v[92:95], v30, s[48:51], s21 offen offset:2048 sc1\n"
"buffer_load_dwordx4 v[140:143], v30, s[48:51], s23 offen offset:2048 sc1\n"
"s_waitcnt vmcnt(0)\n"
"v_max3_u32 v8, v56, v57, v58\n"
"v_max3_u32 v8, v59, v60, v8\n"
"v_max3_u32 v8, v61, v62, v8\n"
"v_max_u32 v8, v63, v8\n"
"v_cmp_eq_u32 vcc, -1, v8\n"
"s_sub_u32 s83, s83, 1\n"
"s_cbranch_scc1 .Lbk84\n"
"s_nop 2\n"
"s_cbranch_vccnz .Lsp135\n"
"s_branch .Lbk84\n"
".Lsl85:\n"
"s_mov_b32 s83, 50000\n"
".Lsp136:\n"
"s_sleep 4\n"
"buffer_load_dwordx4 v[224:227], v30, s[48:51], s26 offen sc1\n"
"buffer_load_dwordx4 v[228:231], v30, s[48:51], s27 offen sc1\n"
"buffer_load_dwordx4 v[232:235], v30, s[48:51], s20 offen offset:1024 sc1\n"
"buffer_load_dwordx4 v[236:239], v30, s[48:51], s21 offen offset:1024 sc1\n"
"buffer_load_dwordx4 v[128:131], v30, s[48:51], s22 offen offset:1024 sc1\n"
"buffer_load_dwordx4 v[132:135], v30, s[48:51], s23 offen offset:1024 sc1\n"
"buffer_load_dwordx4 v[136:139], v30, s[48:51], s24 offen offset:1024 sc1\n"
"buffer_load_dwordx4 v[80:83], v30, s[48:51], s25 offen offset:1024 sc1\n"
"buffer_load_dwordx4 v[84:87], v30, s[48:51], s26 offen offset:1024 sc1\n"
"buffer_load_dwordx4 v[88:91], v30, s[48:51], s27 offen offset:1024 sc1\n"
"buffer_load_dwordx4 v[92:95], v30, s[48:51], s21 offen offset:2048 sc1\n"
"buffer_load_dwordx4 v[140:143], v30, s[48:51], s23 offen offset:2048 sc1\n"
"buffer_load_dwordx4 v[144:147], v30, s[48:51], s25 offen offset:2048 sc1\n"
"buffer_load_dwordx4 v[148:151], v30, s[48:51], s27 offen offset:2048 sc1\n"
"s_waitcnt vmcnt(0)\n"
"v_max3_u32 v8, v224, v225, v226\n"
"v_max3_u32 v8, v227, v228, v8\n"
"v_max3_u32 v8, v229, v230, v8\n"
"v_max_u32 v8, v231, v8\n"
"v_cmp_eq_u32 vcc, -1, v8\n"
"s_sub_u32 s83, s83, 1\n"
"s_cbranch_scc1 .Lbk86\n"
"s_nop 2\n"
"s_cbranch_vccnz .Lsp136\n"
"s_branch .Lbk86\n"
".Lsl87:\n"
"s_mov_b32 s83, 50000\n"
".Lsp137:\n"
"s_sleep 4\n"
"buffer_load_dwordx4 v[232:235], v30, s[48:51], s20 offen offset:1024 sc1\n"
"buffer_load_dwordx4 v[236:239], v30, s[48:51], s21 offen offset:1024 sc1\n"
"buffer_load_dwordx4 v[128:131], v30, s[48:51], s22 offen offset:1024 sc1\n"
"buffer_load_dwordx4 v[132:135], v30, s[48:51], s23 offen offset:1024 sc1\n"
"buffer_load_dwordx4 v[136:139], v30, s[48:51], s24 offen offset:1024 sc1\n"
"buffer_load_dwordx4 v[80:83], v30, s[48:51], s25 offen offset:1024 sc1\n"
"buffer_load_dwordx4 v[84:87], v30, s[48:51], s26 offen offset:1024 sc1\n"
"buffer_load_dwordx4 v[88:91], v30, s[48:51], s27 offen offset:1024 sc1\n"
"buffer_load_dwordx4 v[92:95], v30, s[48:51], s21 offen offset:2048 sc1\n"
"buffer_load_dwordx4 v[140:143], v30, s[48:51], s23 offen offset:2048 sc1\n"
"buffer_load_dwordx4 v[144:147], v30, s[48:51], s25 offen offset:2048 sc1\n"
"buffer_load_dwordx4 v[148:151], v30, s[48:51], s27 offen offset:2048 sc1\n"
"buffer_load_dwordx4 v[152:155], v30, s[48:51], s20 offen offset:2048 sc1\n"
"buffer_load_dwordx4 v[156:159], v30, s[48:51], s22 offen offset:2048 sc1\n"
"s_waitcnt vmcnt(0)\n"
"v_max3_u32 v8, v232, v233, v234\n"
"v_max3_u32 v8, v235, v236, v8\n"
"v_max3_u32 v8, v237, v238, v8\n"
"v_max_u32 v8, v239, v8\n"
"v_cmp_eq_u32 vcc, -1, v8\n"
"s_sub_u32 s83, s83, 1\n"
"s_cbranch_scc1 .Lbk88\n"
"s_nop 2\n"
"s_cbranch_vccnz .Lsp137\n"
"s_branch .Lbk88\n"
".Lsl89:\n"
"s_mov_b32 s83, 50000\n"
".Lsp138:\n"
"s_sleep 4\n"
"buffer_load_dwordx4 v[128:131], v30, s[48:51], s22 offen offset:1024 sc1\n"
"buffer_load_dwordx4 v[132:135], v30, s[48:51], s23 offen offset:1024 sc1\n"
"buffer_load_dwordx4 v[136:139], v30, s[48:51], s24 offen offset:1024 sc1\n"
"buffer_load_dwordx4 v[80:83], v30, s[48:51], s25 offen offset:1024 sc1\n"
"buffer_load_dwordx4 v[84:87], v30, s[48:51], s26 offen offset:1024 sc1\n"
"buffer_load_dwordx4 v[88:91], v30, s[48:51], s27 offen offset:1024 sc1\n"
"buffer_load_dwordx4 v[92:95], v30, s[48:51], s21 offen offset:2048 sc1\n"
"buffer_load_dwordx4 v[140:143], v30, s[48:51], s23 offen offset:2048 sc1\n"
"buffer_load_dwordx4 v[144:147], v30, s[48:51], s25 offen offset:2048 sc1\n"
"buffer_load_dwordx4 v[148:151], v30, s[48:51], s27 offen offset:2048 sc1\n"
"buffer_load_dwordx4 v[152:155], v30, s[48:51], s20 offen offset:2048 sc1\n"
"buffer_load_dwordx4 v[156:159], v30, s[48:51], s22 offen offset:2048 sc1\n"
"buffer_load_dwordx4 v[160:163], v30, s[48:51], s24 offen offset:2048 sc1\n"
"buffer_load_dwordx4 v[164:167], v30, s[48:51], s26 offen offset:2048 sc1\n"
"s_waitcnt vmcnt(0)\n"
"v_max3_u32 v8, v128, v129, v130\n"
"v_max3_u32 v8, v131, v132, v8\n"
"v_max3_u32 v8, v133, v134, v8\n"
"v_max_u32 v8, v135, v8\n"
"v_cmp_eq_u32 vcc, -1, v8\n"
"s_sub_u32 s83, s83, 1\n"
"s_cbranch_scc1 .Lbk90\n"
"s_nop 2\n"
"s_cbranch_vccnz .Lsp138\n"
"s_branch .Lbk90\n"
".Lsl91:\n"
"s_mov_b32 s83, 50000\n"
".Lsp139:\n"
"s_sleep 4\n"
"buffer_load_dwordx4 v[136:139], v30, s[48:51], s24 offen offset:1024 sc1\n"
"buffer_load_dwordx4 v[80:83], v30, s[48:51], s25 offen offset:1024 sc1\n"
"buffer_load_dwordx4 v[84:87], v30, s[48:51], s26 offen offset:1024 sc1\n"
"buffer_load_dwordx4 v[88:91], v30, s[48:51], s27 offen offset:1024 sc1\n"
"buffer_load_dwordx4 v[92:95], v30, s[48:51], s21 offen offset:2048 sc1\n"
"buffer_load_dwordx4 v[140:143], v30, s[48:51], s23 offen offset:2048 sc1\n"
"buffer_load_dwordx4 v[144:147], v30, s[48:51], s25 offen offset:2048 sc1\n"
"buffer_load_dwordx4 v[148:151], v30, s[48:51], s27 offen offset:2048 sc1\n"
"buffer_load_dwordx4 v[152:155], v30, s[48:51], s20 offen offset:2048 sc1\n"
"buffer_load_dwordx4 v[156:159], v30, s[48:51], s22 offen offset:2048 sc1\n"
"buffer_load_dwordx4 v[160:163], v30, s[48:51], s24 offen offset:2048 sc1\n"
"buffer_load_dwordx4 v[164:167], v30, s[48:51], s26 offen offset:2048 sc1\n"
"s_waitcnt vmcnt(0)\n"
"v_max3_u32 v8, v136, v137, v138\n"
"v_max3_u32 v8, v139, v80, v8\n"
"v_max3_u32 v8, v81, v82, v8\n"
"v_max_u32 v8, v83, v8\n"
"v_cmp_eq_u32 vcc, -1, v8\n"
"s_sub_u32 s83, s83, 1\n"
"s_cbranch_scc1 .Lbk92\n"
"s_nop 2\n"
"s_cbranch_vccnz .Lsp139\n"
"s_branch .Lbk92\n"
".Lsl93:\n"
"s_mov_b32 s83, 50000\n"
".Lsp140:\n"
"s_sleep 4\n"
"buffer_load_dwordx4 v[84:87], v30, s[48:51], s26 offen offset:1024 sc1\n"
"buffer_load_dwordx4 v[88:91], v30, s[48:51], s27 offen offset:1024 sc1\n"
"buffer_load_dwordx4 v[92:95], v30, s[48:51], s21 offen offset:2048 sc1\n"
"buffer_load_dwordx4 v[140:143], v30, s[48:51], s23 offen offset:2048 sc1\n"
"buffer_load_dwordx4 v[144:147], v30, s[48:51], s25 offen offset:2048 sc1\n"
"buffer_load_dwordx4 v[148:151], v30, s[48:51], s27 offen offset:2048 sc1\n"
"buffer_load_dwordx4 v[152:155], v30, s[48:51], s20 offen offset:2048 sc1\n"
"buffer_load_dwordx4 v[156:159], v30, s[48:51], s22 offen offset:2048 sc1\n"
"buffer_load_dwordx4 v[160:163], v30, s[48:51], s24 offen offset:2048 sc1\n"
"buffer_load_dwordx4 v[164:167], v30, s[48:51], s26 offen offset:2048 sc1\n"
"s_waitcnt vmcnt(0)\n"
"v_max3_u32 v8, v84, v85, v86\n"
"v_max3_u32 v8, v87, v88, v8\n"
"v_max3_u32 v8, v89, v90, v8\n"
"v_max_u32 v8, v91, v8\n"
"v_cmp_eq_u32 vcc, -1, v8\n"
"s_sub_u32 s83, s83, 1\n"
"s_cbranch_scc1 .Lbk94\n"
"s_nop 2\n"
"s_cbranch_vccnz .Lsp140\n"
"s_branch .Lbk94\n"
".Lsl95:\n"
"s_mov_b32 s83, 50000\n"
".Lsp141:\n"
"s_sleep 4\n"
"buffer_load_dwordx4 v[92:95], v30, s[48:51], s21 offen offset:2048 sc1\n"
"buffer_load_dwordx4 v[140:143], v30, s[48:51], s23 offen offset:2048 sc1\n"
"buffer_load_dwordx4 v[144:147], v30, s[48:51], s25 offen offset:2048 sc1\n"
"buffer_load_dwordx4 v[148:151], v30, s[48:51], s27 offen offset:2048 sc1\n"
"buffer_load_dwordx4 v[152:155], v30, s[48:51], s20 offen offset:2048 sc1\n"
"buffer_load_dwordx4 v[156:159], v30, s[48:51], s22 offen offset:2048 sc1\n"
"buffer_load_dwordx4 v[160:163], v30, s[48:51], s24 offen offset:2048 sc1\n"
"buffer_load_dwordx4 v[164:167], v30, s[48:51], s26 offen offset:2048 sc1\n"
"s_waitcnt vmcnt(0)\n"
"v_max3_u32 v8, v92, v93, v94\n"
"v_max3_u32 v8, v95, v140, v8\n"
"v_max3_u32 v8, v141, v142, v8\n"
"v_max3_u32 v8, v143, v144, v8\n"
"v_max3_u32 v8, v145, v146, v8\n"
"v_max3_u32 v8, v147, v148, v8\n"
"v_max3_u32 v8, v149, v150, v8\n"
"v_max_u32 v8, v151, v8\n"
"v_cmp_eq_u32 vcc, -1, v8\n"
"s_sub_u32 s83, s83, 1\n"
"s_cbranch_scc1 .Lbk96\n"
"s_nop 2\n"
"s_cbranch_vccnz .Lsp141\n"
"s_branch .Lbk96\n"
".Lsl97:\n"
"s_mov_b32 s83, 50000\n"
".Lsp142:\n"
"s_sleep 4\n"
"buffer_load_dwordx4 v[152:155], v30, s[48:51], s20 offen offset:2048 sc1\n"
"buffer_load_dwordx4 v[156:159], v30, s[48:51], s22 offen offset:2048 sc1\n"
"buffer_load_dwordx4 v[160:163], v30, s[48:51], s24 offen offset:2048 sc1\n"
"buffer_load_dwordx4 v[164:167], v30, s[48:51], s26 offen offset:2048 sc1\n"
"s_waitcnt vmcnt(0)\n"
"v_max3_u32 v8, v152, v153, v154\n"
"v_max3_u32 v8, v155, v156, v8\n"
"v_max3_u32 v8, v157, v158, v8\n"
"v_max3_u32 v8, v159, v160, v8\n"
"v_max3_u32 v8, v161, v162, v8\n"
"v_max3_u32 v8, v163, v164, v8\n"
"v_max3_u32 v8, v165, v166, v8\n"
"v_max_u32 v8, v167, v8\n"
"v_cmp_eq_u32 vcc, -1, v8\n"
"s_sub_u32 s83, s83, 1\n"
"s_cbranch_scc1 .Lbk98\n"
"s_nop 2\n"
"s_cbranch_vccnz .Lsp142\n"
"s_branch .Lbk98\n"
".Lcode1:\n"
      :
      : "s"(ka), "s"(bid), "s"(wave)
      : "v0","v1","v2","v3","v4","v5","v6","v7","v8","v9","v10","v11","v12","v13","v14","v15","v16","v17","v18","v19","v20","v21","v22","v23","v24","v25","v26","v27","v28","v29","v30","v31","v32","v33","v34","v35","v36","v37","v38","v39","v40","v41","v42","v43","v44","v45","v46","v47","v48","v49","v50","v51","v52","v53","v54","v55","v56","v57","v58","v59","v60","v61","v62","v63","v64","v65","v66","v67","v68","v69","v70","v71","v72","v73","v74","v75","v76","v77","v78","v79","v80","v81","v82","v83","v84","v85","v86","v87","v88","v89","v90","v91","v92","v93","v94","v95","v96","v97","v98","v99","v100","v101","v102","v103","v104","v105","v106","v107","v108","v109","v110","v111","v112","v113","v114","v115","v116","v117","v118","v119","v120","v121","v122","v123","v124","v125","v126","v127","v128","v129","v130","v131","v132","v133","v134","v135","v136","v137","v138","v139","v140","v141","v142","v143","v144","v145","v146","v147","v148","v149","v150","v151","v152","v153","v154","v155","v156","v157","v158","v159","v160","v161","v162","v163","v164","v165","v166","v167","v168","v169","v170","v171","v172","v173","v174","v175","v176","v177","v178","v179","v180","v181","v182","v183","v184","v185","v186","v187","v188","v189","v190","v191","v192","v193","v194","v195","v196","v197","v198","v199","v200","v201","v202","v203","v204","v205","v206","v207","v208","v209","v210","v211","v212","v213","v214","v215","v216","v217","v218","v219","v220","v221","v222","v223","v224","v225","v226","v227","v228","v229","v230","v231","v232","v233","v234","v235","v236","v237","v238","v239","v240","v241","v242","v243","v244","v245","v246","v247","v248","v249","v250","v251","v252","v253","v254","v255","a0","a1","a2","a3","a4","a5","a6","a7","a8","a9","a10","a11","a12","a13","a14","a15","a16","a17","a18","a19","a20","a21","a22","a23","a24","a25","a26","a27","a28","a29","a30","a31","a32","a33","a34","a35","a36","a37","a38","a39","a40","a41","a42","a43","a44","a45","a46","a47","a48","a49","a50","a51","a52","a53","a54","a55","a56","a57","a58","a59","a60","a61","a62","a63","a64","a65","a66","a67","a68","a69","a70","a71","a72","a73","a74","a75","a76","a77","a78","a79","a80","a81","a82","a83","a84","a85","a86","a87","a88","a89","a90","a91","a92","a93","a94","a95","a96","a97","a98","a99","a100","a101","a102","a103","a104","a105","a106","a107","a108","a109","a110","a111","a112","a113","a114","a115","a116","a117","a118","a119","a120","a121","a122","a123","a124","a125","a126","a127","a128","a129","a130","a131","a132","a133","a134","a135","a136","a137","a138","a139","a140","a141","a142","a143","a144","a145","a146","a147","a148","a149","a150","a151","a152","a153","a154","a155","a156","a157","a158","a159","a160","a161","a162","a163","a164","a165","a166","a167","a168","a169","a170","a171","a172","a173","a174","a175","a176","a177","a178","a179","a180","a181","a182","a183","a184","a185","a186","a187","a188","a189","a190","a191","a192","a193","a194","a195","a196","a197","a198","a199","a200","a201","a202","a203","a204","a205","a206","a207","a208","a209","a210","a211","a212","a213","a214","a215","a216","a217","a218","a219","a220","a221","a222","a223","a224","a225","a226","a227","a228","a229","a230","a231","a232","a233","a234","a235","a236","a237","a238","a239","a240","a241","a242","a243","a244","a245","a246","a247","a248","a249","a250","a251","a252","a253","a254","a255","s18","s19","s20","s21","s22","s23","s24","s25","s26","s27","s28","s29","s30","s31","s32","s33","s34","s35","s36","s37","s38","s39","s40","s41","s42","s43","s44","s45","s46","s47","s48","s49","s50","s51","s52","s53","s54","s55","s56","s57","s58","s59","s60","s61","s62","s63","s64","s65","s66","s67","s68","s69","s70","s71","s72","s73","s74","s75","s76","s77","s78","s79","s80","s81","s82","s83","s84","s85","s86","s87","s88","s89","s90","s91","s92","s93","s94","s95","s96","s97","s98","s99","s100","s101","vcc","m0","scc","memory");
}
extern "C" void kernel_launch(void* const* d_in, const int* in_sizes, int n_in, void* d_out, int out_size, void* d_ws,
                              size_t ws_size, hipStream_t stream) {
  (void)in_sizes; (void)n_in; (void)out_size; (void)ws_size;
  (void)hipFuncSetAttribute((const void*)fused_block_kernel, hipFuncAttributeMaxDynamicSharedMemorySize, 163840);
  fused_block_kernel<<<256, 256, 163840, stream>>>(d_in[0], d_in[1], d_in[2], d_in[4], d_in[5], d_in[6], d_out, d_ws);
}
```
